```python
import torch

def _softmax(x, axis=-1):
    return torch.softmax(x, dim=axis)

def _gelu(x):
    return torch.nn.functional.gelu(x, approximate='tanh')

def _silu(x):
    return torch.nn.functional.silu(x)

def _sigmoid(x):
    return torch.sigmoid(x)

def _layernorm(x, axis=-1, eps=1e-05):
    m = x.mean(dim=axis, keepdim=True)
    v = x.var(dim=axis, keepdim=True, unbiased=False)
    return (x - m) / torch.sqrt(v + eps)

def _scatter_add(tgt, idx, src, axis=0):
    return tgt.clone().index_add_(axis, idx, src)

def _segment_sum(src, seg, n):
    out = torch.zeros((n,) + src.shape[1:], dtype=src.dtype)
    return out.index_add_(0, seg, src)

def _pad(x, pw):
    flat = tuple((v for pair in reversed(pw) for v in pair))
    return torch.nn.functional.pad(x, flat)
kernel_name = 'standalone__video_video_swin__vision'

def setup_inputs():

    _w = lambda a: (lambda t: t.to(torch.bfloat16) if t.is_floating_point() else t)(torch.as_tensor(a))
    T = 4
    H = W = 224 // 8 * 8
    C, win = (96, 8)
    return {'x': _w((lambda *s: torch.randn(*s, dtype=torch.bfloat16) * 0.1)(1, T, H, W, C)), 'wqkv_t': _w((lambda *s: torch.randn(*s, dtype=torch.bfloat16) * 0.1)(C, 3 * C)), 'wo_t': _w((lambda *s: torch.randn(*s, dtype=torch.bfloat16) * 0.1)(C, C)), 'wqkv_s': _w((lambda *s: torch.randn(*s, dtype=torch.bfloat16) * 0.1)(C, 3 * C)), 'wo_s': _w((lambda *s: torch.randn(*s, dtype=torch.bfloat16) * 0.8)(C, C))}

def reference(x, wqkv_t, wo_t, wqkv_s, wo_s):
    B, T, H, W, C = x.shape
    win = 8
    scale = C ** (-0.5)
    h = _layernorm(x, axis=-1)
    ht = (lambda x, *d: x.permute(*d))(h, 0, 2, 3, 1, 4).reshape(B * H * W, T, C)
    qkv = ht @ wqkv_t
    q, k, v = (qkv[..., :C].clone(), qkv[..., C:2 * C].clone(), qkv[..., 2 * C:].clone())
    p = _softmax(torch.einsum('btc,bsc->bts', q, k) * scale, axis=-1)
    ot = torch.einsum('bts,bsc->btc', p, v) @ wo_t
    ot = (lambda x, *d: x.permute(*d))(ot.reshape(B, H, W, T, C), 0, 3, 1, 2, 4)
    x = x + ot
    h = _layernorm(x, axis=-1)
    nh, nw = (H // win, W // win)
    hs = h.reshape(B * T, nh, win, nw, win, C)
    hw = (lambda x, *d: x.permute(*d))(hs, 0, 1, 3, 2, 4, 5).reshape(B * T * nh * nw, win * win, C)
    qkv = hw @ wqkv_s
    q, k, v = (qkv[..., :C].clone(), qkv[..., C:2 * C].clone(), qkv[..., 2 * C:].clone())
    p = _softmax(torch.einsum('bqc,bkc->bqk', q, k) * scale, axis=-1)
    os = torch.einsum('bqk,bkc->bqc', p, v) @ wo_s
    os = (lambda x, *d: x.permute(*d))(os.reshape(B * T, nh, nw, win, win, C), 0, 1, 3, 2, 4, 5)
    os = os.reshape(B, T, H, W, C)
    return x + os
```

```cpp
#include <hip/hip_runtime.h>
#include <hip/hip_bf16.h>
#include <stdint.h>

using bf16x8 = __attribute__((ext_vector_type(8))) short;
using f32x16 = __attribute__((ext_vector_type(16))) float;
using u32x4 = __attribute__((ext_vector_type(4))) unsigned;
using f32x2 = __attribute__((ext_vector_type(2))) float;
using bf2 = __attribute__((ext_vector_type(2))) __bf16;
#define DI __device__ __forceinline__
#define MFMA(a, b, c) __builtin_amdgcn_mfma_f32_32x32x16_bf16((a), (b), (c), 0, 0, 0)

constexpr int CH = 96, TT = 4, HH = 224, WW = 224;
constexpr int ROWB = CH * 2;
constexpr int FRAMEB = HH * WW * ROWB;
constexpr int IMGROWB = WW * ROWB;
constexpr int NWIN = 28;
constexpr int NUNITS = NWIN * NWIN;
constexpr int WMAT_BYTES = 18 * 1024;
constexpr float LN_EPS = 1e-5f;

DI unsigned pk2(float a, float b) {
  f32x2 v = {a, b};
  return __builtin_bit_cast(unsigned, __builtin_convertvector(v, bf2));
}
DI float lo16(unsigned u) { return __uint_as_float(u << 16); }
DI float hi16(unsigned u) { return __uint_as_float(u & 0xffff0000u); }
DI bf16x8 asfrag(u32x4 v) { return __builtin_bit_cast(bf16x8, v); }
DI float xhalf_sum(float v) {
  auto r = __builtin_amdgcn_permlane32_swap(__float_as_uint(v), __float_as_uint(v), false, false);
  return __uint_as_float(r[0]) + __uint_as_float(r[1]);
}
DI float xhalf_max(float v) {
  auto r = __builtin_amdgcn_permlane32_swap(__float_as_uint(v), __float_as_uint(v), false, false);
  return __builtin_fmaxf(__uint_as_float(r[0]), __uint_as_float(r[1]));
}
DI u32x4 pack8(const f32x16& a, int s) {
  u32x4 r;
  r[0] = pk2(a[8 * s + 0], a[8 * s + 1]);
  r[1] = pk2(a[8 * s + 2], a[8 * s + 3]);
  r[2] = pk2(a[8 * s + 4], a[8 * s + 5]);
  r[3] = pk2(a[8 * s + 6], a[8 * s + 7]);
  return r;
}
DI f32x16 unpack16(const u32x4& a, const u32x4& b) {
  f32x16 r;
#pragma unroll
  for (int i = 0; i < 4; ++i) {
    r[2 * i] = lo16(a[i]); r[2 * i + 1] = hi16(a[i]);
    r[8 + 2 * i] = lo16(b[i]); r[8 + 2 * i + 1] = hi16(b[i]);
  }
  return r;
}

__global__ void fold_kernel(const __hip_bfloat16* __restrict__ wqkv_t, const __hip_bfloat16* __restrict__ wo_t,
                            const __hip_bfloat16* __restrict__ wqkv_s, const __hip_bfloat16* __restrict__ wo_s,
                            __hip_bfloat16* __restrict__ wf) {
  int idx = blockIdx.x * blockDim.x + threadIdx.x;
  if (idx >= 4 * 9216) return;
  int mat = idx / 9216, e = idx % 9216;
  int frag = e / 512, w = e % 512;
  int lane = w / 8, j = w % 8;
  int T = frag / 6, s = frag % 6;
  int r = lane & 31, hh = lane >> 5;
  int cin = 16 * s + 8 * hh + j;
  int cout = 32 * T + ((r & 0x13) | ((r & 4) << 1) | ((r & 8) >> 1));
  const __hip_bfloat16* wqkv = (mat < 2) ? wqkv_t : wqkv_s;
  const __hip_bfloat16* wo = (mat < 2) ? wo_t : wo_s;
  float acc = 0.f;
  if ((mat & 1) == 0) {
    for (int d = 0; d < CH; ++d)
      acc += __bfloat162float(wqkv[cin * 288 + d]) * __bfloat162float(wqkv[cout * 288 + 96 + d]);
    acc *= 0.10206207261596575f * 1.4426950408889634f;
  } else {
    for (int d = 0; d < CH; ++d)
      acc += __bfloat162float(wqkv[cin * 288 + 192 + d]) * __bfloat162float(wo[d * 96 + cout]);
  }
  wf[idx] = __float2bfloat16(acc);
}

DI void layernorm_tile(const u32x4 (&xf)[6], f32x16 (&xs)[3], u32x4 (&hf)[6]) {
#pragma unroll
  for (int T = 0; T < 3; ++T) xs[T] = unpack16(xf[2 * T], xf[2 * T + 1]);
  float s0 = 0.f, s1 = 0.f, q0 = 0.f, q1 = 0.f;
#pragma unroll
  for (int T = 0; T < 3; ++T)
#pragma unroll
    for (int i = 0; i < 16; i += 2) {
      s0 += xs[T][i]; s1 += xs[T][i + 1];
      q0 = __builtin_fmaf(xs[T][i], xs[T][i], q0); q1 = __builtin_fmaf(xs[T][i + 1], xs[T][i + 1], q1);
    }
  float s = xhalf_sum(s0 + s1), q = xhalf_sum(q0 + q1);
  float m = s * (1.f / CH);
  float var = __builtin_fmaxf(__builtin_fmaf(-m, m, q * (1.f / CH)), 0.f);
  float rs = __builtin_amdgcn_rsqf(var + LN_EPS);
  float nb = -m * rs;
#pragma unroll
  for (int T = 0; T < 3; ++T)
#pragma unroll
    for (int h = 0; h < 2; ++h)
#pragma unroll
      for (int i = 0; i < 4; ++i)
        hf[2 * T + h][i] = pk2(__builtin_fmaf(xs[T][8 * h + 2 * i], rs, nb), __builtin_fmaf(xs[T][8 * h + 2 * i + 1], rs, nb));
}

typedef __attribute__((address_space(3))) char lds_char;
DI u32x4 lds_ld(const lds_char* p) { return *(const __attribute__((address_space(3))) u32x4*)p; }
DI void lds_st(lds_char* p, u32x4 v) { *(__attribute__((address_space(3))) u32x4*)p = v; }

constexpr int XREG = 2 * 6144 + 32;
constexpr int LDS_W = 0;
constexpr int LDS_X = 4 * WMAT_BYTES;
constexpr int LDS_TOTAL = LDS_X + 4 * XREG;

__global__ __launch_bounds__(256, 1) void swin_kernel(const char* __restrict__ x, const char* __restrict__ wf,
                                                      char* __restrict__ out) {
  extern __shared__ __attribute__((aligned(16))) char smem_[];
  lds_char* smem = (lds_char*)smem_;
  const int tid = threadIdx.x, lane = tid & 63;
  const int wid = __builtin_amdgcn_readfirstlane(tid >> 6);
  const int r = lane & 31, hh = lane >> 5;
  const int unit = blockIdx.x;
  const int wy = unit / NWIN, wx = unit % NWIN;

  for (int i = tid; i < 4 * WMAT_BYTES / 16; i += 256)
    lds_st(smem + LDS_W + i * 16, *(const u32x4*)(wf + i * 16));

  const char* xu = x + (size_t)(wy * 8) * IMGROWB + (size_t)(wx * 8) * ROWB;
  const int p = r >> 2, f = r & 3;
  const unsigned xoff = f * FRAMEB + p * ROWB + hh * 16;
  u32x4 xin[2][6];
#pragma unroll
  for (int i = 0; i < 2; ++i)
#pragma unroll
    for (int s = 0; s < 6; ++s)
      xin[i][s] = *(const u32x4*)(xu + (size_t)(2 * wid + i) * IMGROWB + xoff + 32 * s);
  __syncthreads();

  const lds_char* wl = smem + LDS_W + lane * 16;
  const f32x16 zero = {};
  const bool sel0 = (p >> 1) & 1, sel1 = (p >> 2) & 1;
  const bool valid = (hh == (p & 1));
  const int grp = p >> 1;

#pragma unroll
  for (int i = 0; i < 2; ++i) {
    f32x16 xs[3]; u32x4 hf[6];
    layernorm_tile(xin[i], xs, hf);
    f32x16 sa = zero;
#pragma unroll
    for (int T = 0; T < 3; ++T) {
      f32x16 g = zero;
#pragma unroll
      for (int s = 0; s < 6; ++s) g = MFMA(asfrag(lds_ld(wl + 0 * WMAT_BYTES + (T * 6 + s) * 1024)), asfrag(hf[s]), g);
      sa = MFMA(asfrag(hf[2 * T]), asfrag(pack8(g, 0)), sa);
      sa = MFMA(asfrag(hf[2 * T + 1]), asfrag(pack8(g, 1)), sa);
    }
    float e[4];
#pragma unroll
    for (int k = 0; k < 4; ++k) {
      float a = sel0 ? sa[4 + k] : sa[k];
      float b = sel0 ? sa[12 + k] : sa[8 + k];
      e[k] = sel1 ? b : a;
    }
    float mx = __builtin_fmaxf(__builtin_fmaxf(e[0], e[1]), __builtin_fmaxf(e[2], e[3]));
#pragma unroll
    for (int k = 0; k < 4; ++k) e[k] = __builtin_amdgcn_exp2f(e[k] - mx);
    float rinv = __builtin_amdgcn_rcpf((e[0] + e[1]) + (e[2] + e[3]));
    rinv = valid ? rinv : 0.f;
    unsigned pk0 = pk2(e[0] * rinv, e[1] * rinv), pk1 = pk2(e[2] * rinv, e[3] * rinv);
    u32x4 pf[2];
#pragma unroll
    for (int g = 0; g < 4; ++g) {
      pf[g >> 1][(g & 1) * 2 + 0] = (grp == g) ? pk0 : 0u;
      pf[g >> 1][(g & 1) * 2 + 1] = (grp == g) ? pk1 : 0u;
    }
    lds_char* xw = smem + LDS_X + f * XREG + i * 6144 + (hh * 32 + 8 * wid + p) * 16;
#pragma unroll
    for (int T = 0; T < 3; ++T) {
      f32x16 v = zero;
#pragma unroll
      for (int s = 0; s < 6; ++s) v = MFMA(asfrag(hf[s]), asfrag(lds_ld(wl + 1 * WMAT_BYTES + (T * 6 + s) * 1024)), v);
      f32x16 o = xs[T];
      o = MFMA(asfrag(pack8(v, 0)), asfrag(pf[0]), o);
      o = MFMA(asfrag(pack8(v, 1)), asfrag(pf[1]), o);
      lds_st(xw + (2 * T) * 1024, pack8(o, 0));
      lds_st(xw + (2 * T + 1) * 1024, pack8(o, 1));
    }
  }
  __syncthreads();

  lds_char* xr = smem + LDS_X + wid * XREG;
  u32x4 x1[2][6], h2[2][6];
#pragma unroll
  for (int i = 0; i < 2; ++i) {
#pragma unroll
    for (int s = 0; s < 6; ++s) x1[i][s] = lds_ld(xr + i * 6144 + s * 1024 + lane * 16);
    f32x16 xs[3];
    layernorm_tile(x1[i], xs, h2[i]);
  }
  u32x4 pf[2][4];
#pragma unroll
  for (int q = 0; q < 2; ++q) {
    f32x16 sa0 = zero, sa1 = zero;
#pragma unroll
    for (int T = 0; T < 3; ++T) {
      f32x16 g = zero;
#pragma unroll
      for (int s = 0; s < 6; ++s) g = MFMA(asfrag(lds_ld(wl + 2 * WMAT_BYTES + (T * 6 + s) * 1024)), asfrag(h2[q][s]), g);
      u32x4 g0 = pack8(g, 0), g1 = pack8(g, 1);
      sa0 = MFMA(asfrag(h2[0][2 * T]), asfrag(g0), sa0);
      sa1 = MFMA(asfrag(h2[1][2 * T]), asfrag(g0), sa1);
      sa0 = MFMA(asfrag(h2[0][2 * T + 1]), asfrag(g1), sa0);
      sa1 = MFMA(asfrag(h2[1][2 * T + 1]), asfrag(g1), sa1);
    }
    float m0 = __builtin_fmaxf(sa0[0], sa1[0]), m1 = __builtin_fmaxf(sa0[1], sa1[1]);
#pragma unroll
    for (int k = 2; k < 16; k += 2) {
      m0 = __builtin_fmaxf(__builtin_fmaxf(m0, sa0[k]), sa1[k]);
      m1 = __builtin_fmaxf(__builtin_fmaxf(m1, sa0[k + 1]), sa1[k + 1]);
    }
    float mx = xhalf_max(__builtin_fmaxf(m0, m1));
    float l0 = 0.f, l1 = 0.f;
#pragma unroll
    for (int k = 0; k < 16; ++k) {
      sa0[k] = __builtin_amdgcn_exp2f(sa0[k] - mx); l0 += sa0[k];
      sa1[k] = __builtin_amdgcn_exp2f(sa1[k] - mx); l1 += sa1[k];
    }
    float rinv = __builtin_amdgcn_rcpf(xhalf_sum(l0 + l1));
#pragma unroll
    for (int k = 0; k < 16; ++k) { sa0[k] *= rinv; sa1[k] *= rinv; }
    pf[q][0] = pack8(sa0, 0); pf[q][1] = pack8(sa0, 1);
    pf[q][2] = pack8(sa1, 0); pf[q][3] = pack8(sa1, 1);
  }
#pragma unroll
  for (int T = 0; T < 3; ++T) {
    f32x16 v0 = zero, v1 = zero;
#pragma unroll
    for (int s = 0; s < 6; ++s) {
      bf16x8 w = asfrag(lds_ld(wl + 3 * WMAT_BYTES + (T * 6 + s) * 1024));
      v0 = MFMA(asfrag(h2[0][s]), w, v0);
      v1 = MFMA(asfrag(h2[1][s]), w, v1);
    }
    u32x4 vf[4] = {pack8(v0, 0), pack8(v0, 1), pack8(v1, 0), pack8(v1, 1)};
#pragma unroll
    for (int q = 0; q < 2; ++q) {
      f32x16 o = unpack16(x1[q][2 * T], x1[q][2 * T + 1]);
#pragma unroll
      for (int k = 0; k < 4; ++k) o = MFMA(asfrag(vf[k]), asfrag(pf[q][k]), o);
      x1[q][2 * T] = pack8(o, 0);
      x1[q][2 * T + 1] = pack8(o, 1);
    }
  }
  char* ou = out + (size_t)wid * FRAMEB + (size_t)(wy * 8) * IMGROWB + (size_t)(wx * 8) * ROWB;
#pragma unroll
  for (int q = 0; q < 2; ++q) {
    lds_char* st = xr + q * 6144;
#pragma unroll
    for (int s = 0; s < 6; ++s) {
      int c = 2 * s + hh;
      lds_st(st + r * ROWB + 16 * (c ^ ((r >> 1) & 3)), x1[q][s]);
    }
  }
  __builtin_amdgcn_s_waitcnt(0xC07F);
  asm volatile("" ::: "memory");
#pragma unroll
  for (int q = 0; q < 2; ++q) {
#pragma unroll
    for (int i = 0; i < 6; ++i) {
      int ch = i * 64 + lane;
      int tok = ch / 12, cp = ch % 12;
      int c = cp ^ ((tok >> 1) & 3);
      u32x4 v = lds_ld(xr + q * 6144 + ch * 16);
      *(u32x4*)(ou + (size_t)(2 * (tok >> 3) + q) * IMGROWB + (tok & 7) * ROWB + c * 16) = v;
    }
  }
}

extern "C" void kernel_launch(void* const* d_in, const int* in_sizes, int n_in, void* d_out, int out_size,
                              void* d_ws, size_t ws_size, hipStream_t stream) {
  const __hip_bfloat16* x = (const __hip_bfloat16*)d_in[0];
  const __hip_bfloat16* wqkv_t = (const __hip_bfloat16*)d_in[1];
  const __hip_bfloat16* wo_t = (const __hip_bfloat16*)d_in[2];
  const __hip_bfloat16* wqkv_s = (const __hip_bfloat16*)d_in[3];
  const __hip_bfloat16* wo_s = (const __hip_bfloat16*)d_in[4];
  __hip_bfloat16* wf = (__hip_bfloat16*)d_ws;
  fold_kernel<<<(4 * 9216 + 255) / 256, 256, 0, stream>>>(wqkv_t, wo_t, wqkv_s, wo_s, wf);
  hipFuncSetAttribute((const void*)swin_kernel, hipFuncAttributeMaxDynamicSharedMemorySize, LDS_TOTAL);
  swin_kernel<<<NUNITS, 256, LDS_TOTAL, stream>>>((const char*)x, (const char*)wf, (char*)d_out);
}
```

```cpp
#include <hip/hip_runtime.h>
#include <hip/hip_bf16.h>
#include <stdint.h>

using bf16x8 = __attribute__((ext_vector_type(8))) short;
using f32x16 = __attribute__((ext_vector_type(16))) float;
using u32x4 = __attribute__((ext_vector_type(4))) unsigned;
using f32x2 = __attribute__((ext_vector_type(2))) float;
using bf2 = __attribute__((ext_vector_type(2))) __bf16;
#if defined(__HIP_DEVICE_COMPILE__)
#define NOPK __attribute__((target("no-packed-fp32-ops")))
#else
#define NOPK
#endif
#define DI __device__ __forceinline__ NOPK
#define MFMA(a, b, c) __builtin_amdgcn_mfma_f32_32x32x16_bf16((a), (b), (c), 0, 0, 0)

constexpr int CH = 96, HH = 224, WW = 224;
constexpr int ROWB = CH * 2;
constexpr int FRAMEB = HH * WW * ROWB;
constexpr int IMGROWB = WW * ROWB;
constexpr int NWIN = 28;
constexpr int NUNITS = NWIN * NWIN;
constexpr int WMAT_BYTES = 18 * 1024;
constexpr float LN_EPS = 1e-5f;
constexpr int NWG = 256;

DI unsigned pk2(float a, float b) {
  f32x2 v = {a, b};
  return __builtin_bit_cast(unsigned, __builtin_convertvector(v, bf2));
}
DI float lo16(unsigned u) { return __builtin_bit_cast(float, u << 16); }
DI float hi16(unsigned u) { return __builtin_bit_cast(float, u & 0xffff0000u); }
DI unsigned fbits(float f) { return __builtin_bit_cast(unsigned, f); }
DI float bitsf(unsigned u) { return __builtin_bit_cast(float, u); }
DI bf16x8 asfrag(u32x4 v) { return __builtin_bit_cast(bf16x8, v); }
DI float xhalf_sum(float v) {
  auto r = __builtin_amdgcn_permlane32_swap(fbits(v), fbits(v), false, false);
  return bitsf(r[0]) + bitsf(r[1]);
}
DI float xhalf_max(float v) {
  auto r = __builtin_amdgcn_permlane32_swap(fbits(v), fbits(v), false, false);
  return __builtin_fmaxf(bitsf(r[0]), bitsf(r[1]));
}
DI float bsel(unsigned m, float a, float b) { return bitsf((m & fbits(a)) | (~m & fbits(b))); }
DI u32x4 pack8(const f32x16& a, int s) {
  u32x4 r;
  r[0] = pk2(a[8 * s + 0], a[8 * s + 1]);
  r[1] = pk2(a[8 * s + 2], a[8 * s + 3]);
  r[2] = pk2(a[8 * s + 4], a[8 * s + 5]);
  r[3] = pk2(a[8 * s + 6], a[8 * s + 7]);
  return r;
}
DI f32x16 unpack16(const u32x4& a, const u32x4& b) {
  f32x16 r;
#pragma unroll
  for (int i = 0; i < 4; ++i) {
    r[2 * i] = lo16(a[i]); r[2 * i + 1] = hi16(a[i]);
    r[8 + 2 * i] = lo16(b[i]); r[8 + 2 * i + 1] = hi16(b[i]);
  }
  return r;
}

__global__ void fold_kernel(const __hip_bfloat16* __restrict__ wqkv_t, const __hip_bfloat16* __restrict__ wo_t,
                            const __hip_bfloat16* __restrict__ wqkv_s, const __hip_bfloat16* __restrict__ wo_s,
                            __hip_bfloat16* __restrict__ wf) {
  int idx = blockIdx.x * blockDim.x + threadIdx.x;
  if (idx >= 4 * 9216) return;
  int mat = idx / 9216, e = idx % 9216;
  int frag = e / 512, w = e % 512;
  int lane = w / 8, j = w % 8;
  int T = frag / 6, s = frag % 6;
  int r = lane & 31, hh = lane >> 5;
  int cin = 16 * s + 8 * hh + j;
  int cout = 32 * T + ((r & 0x13) | ((r & 4) << 1) | ((r & 8) >> 1));
  const __hip_bfloat16* wqkv = (mat < 2) ? wqkv_t : wqkv_s;
  const __hip_bfloat16* wo = (mat < 2) ? wo_t : wo_s;
  float acc = 0.f;
  if ((mat & 1) == 0) {
    for (int d = 0; d < CH; ++d)
      acc += __bfloat162float(wqkv[cin * 288 + d]) * __bfloat162float(wqkv[cout * 288 + 96 + d]);
    acc *= 0.10206207261596575f * 1.4426950408889634f;
  } else {
    for (int d = 0; d < CH; ++d)
      acc += __bfloat162float(wqkv[cin * 288 + 192 + d]) * __bfloat162float(wo[d * 96 + cout]);
  }
  wf[idx] = __float2bfloat16(acc);
}

DI void layernorm_tile(const u32x4 (&xf)[6], f32x16 (&xs)[3], u32x4 (&hf)[6]) {
#pragma unroll
  for (int T = 0; T < 3; ++T) xs[T] = unpack16(xf[2 * T], xf[2 * T + 1]);
  float s0 = xs[0][0], s1 = xs[0][1], q0 = xs[0][0] * xs[0][0], q1 = xs[0][1] * xs[0][1];
#pragma unroll
  for (int T = 0; T < 3; ++T)
#pragma unroll
    for (int i = (T == 0 ? 2 : 0); i < 16; i += 2) {
      s0 += xs[T][i]; s1 += xs[T][i + 1];
      q0 = __builtin_fmaf(xs[T][i], xs[T][i], q0); q1 = __builtin_fmaf(xs[T][i + 1], xs[T][i + 1], q1);
    }
  float s = xhalf_sum(s0 + s1), q = xhalf_sum(q0 + q1);
  float m = s * (1.f / CH);
  float var = __builtin_fmaxf(__builtin_fmaf(-m, m, q * (1.f / CH)), 0.f);
  float rs = __builtin_amdgcn_rsqf(var + LN_EPS);
  float nb = -m * rs;
#pragma unroll
  for (int T = 0; T < 3; ++T)
#pragma unroll
    for (int h = 0; h < 2; ++h)
#pragma unroll
      for (int i = 0; i < 4; ++i)
        hf[2 * T + h][i] = pk2(__builtin_fmaf(xs[T][8 * h + 2 * i], rs, nb), __builtin_fmaf(xs[T][8 * h + 2 * i + 1], rs, nb));
}

typedef __attribute__((address_space(3))) char lds_char;
typedef __attribute__((address_space(3))) unsigned lds_u32;
DI u32x4 lds_ld(const lds_char* p) { return *(const __attribute__((address_space(3))) u32x4*)p; }
DI void lds_st(lds_char* p, u32x4 v) { *(__attribute__((address_space(3))) u32x4*)p = v; }

DI void grp_arrive(lds_u32* cnt, int lane) {
  asm volatile("s_waitcnt lgkmcnt(0)" ::: "memory");
  if (lane == 0) __hip_atomic_fetch_add(cnt, 1u, __ATOMIC_RELAXED, __HIP_MEMORY_SCOPE_WORKGROUP);
  asm volatile("" ::: "memory");
}
DI void grp_wait(lds_u32* cnt, unsigned target) {
  asm volatile("" ::: "memory");
  for (;;) {
    unsigned v = __builtin_amdgcn_readfirstlane(__hip_atomic_load(cnt, __ATOMIC_RELAXED, __HIP_MEMORY_SCOPE_WORKGROUP));
    if (v >= target) break;
    __builtin_amdgcn_s_sleep(1);
  }
  asm volatile("" ::: "memory");
}

constexpr int OROW = 1792;
constexpr int OTOK = 208;
constexpr int XREG = 4 * OROW + 32;
constexpr int LDS_W = 0;
constexpr int LDS_X = 4 * WMAT_BYTES;
constexpr int LDS_CNT = LDS_X + 8 * XREG;
constexpr int LDS_TOTAL = LDS_CNT + 64;

#define STAMP(i) do {} while (0)
struct TMasks { unsigned s0, s1, g[4]; };

DI void temporal_scores(const lds_char* wl, const u32x4 (&hf)[6], const TMasks& mk, u32x4 (&pf)[2]) {
  const f32x16 zero = {};
  f32x16 sa = zero;
#pragma unroll
  for (int T = 0; T < 3; ++T) {
    f32x16 g = zero;
#pragma unroll
    for (int s = 0; s < 6; ++s) g = MFMA(asfrag(lds_ld(wl + 0 * WMAT_BYTES + (T * 6 + s) * 1024)), asfrag(hf[s]), g);
    sa = MFMA(asfrag(hf[2 * T]), asfrag(pack8(g, 0)), sa);
    sa = MFMA(asfrag(hf[2 * T + 1]), asfrag(pack8(g, 1)), sa);
  }
  float e[4];
#pragma unroll
  for (int k = 0; k < 4; ++k) {
    float a = bsel(mk.s0, sa[4 + k], sa[k]);
    float b = bsel(mk.s0, sa[12 + k], sa[8 + k]);
    e[k] = bsel(mk.s1, b, a);
  }
  float mx = __builtin_fmaxf(__builtin_fmaxf(e[0], e[1]), __builtin_fmaxf(e[2], e[3]));
#pragma unroll
  for (int k = 0; k < 4; ++k) e[k] = __builtin_amdgcn_exp2f(e[k] - mx);
  float rinv = __builtin_amdgcn_rcpf((e[0] + e[1]) + (e[2] + e[3]));
  unsigned pk0 = pk2(e[0] * rinv, e[1] * rinv), pk1 = pk2(e[2] * rinv, e[3] * rinv);
#pragma unroll
  for (int g = 0; g < 4; ++g) {
    pf[g >> 1][(g & 1) * 2 + 0] = pk0 & mk.g[g];
    pf[g >> 1][(g & 1) * 2 + 1] = pk1 & mk.g[g];
  }
}
DI void temporal_out(const lds_char* wl, const u32x4 (&hf)[6], const u32x4 (&pf)[2], const f32x16 (&xs)[3], u32x4 (&o8)[6]) {
  const f32x16 zero = {};
#pragma unroll
  for (int T = 0; T < 3; ++T) {
    f32x16 v = zero;
#pragma unroll
    for (int s = 0; s < 6; ++s) v = MFMA(asfrag(hf[s]), asfrag(lds_ld(wl + 1 * WMAT_BYTES + (T * 6 + s) * 1024)), v);
    f32x16 o = xs[T];
    o = MFMA(asfrag(pack8(v, 0)), asfrag(pf[0]), o);
    o = MFMA(asfrag(pack8(v, 1)), asfrag(pf[1]), o);
    o8[2 * T] = pack8(o, 0);
    o8[2 * T + 1] = pack8(o, 1);
  }
}

DI void load_tile(const char* xrow, unsigned xoff, u32x4 (&xf)[6]) {
#pragma unroll
  for (int s = 0; s < 6; ++s) xf[s] = *(const u32x4*)(xrow + xoff + 32 * s);
}

__global__ __launch_bounds__(512, 2) NOPK void swin_kernel(const char* __restrict__ x, const char* __restrict__ wf,
                                                           char* __restrict__ out) {
  extern __shared__ __attribute__((aligned(16))) char smem_[];
  lds_char* smem = (lds_char*)smem_;
  const int tid = threadIdx.x, lane = tid & 63;
  const int wid8 = __builtin_amdgcn_readfirstlane(tid >> 6);
  const int grp = wid8 >> 2, wid = wid8 & 3;
  const int r = lane & 31, hh = lane >> 5;
  const int p = r >> 2, f = r & 3;
  const unsigned xoff = f * FRAMEB + p * ROWB + hh * 16;

  int unit = blockIdx.x + NWG * grp;
  STAMP(0);
  u32x4 xin0[6], xin1[6];
  {
    const int wy = unit / NWIN, wx = unit % NWIN;
    const char* xu = x + (size_t)(wy * 8 + 2 * wid) * IMGROWB + (size_t)(wx * 8) * ROWB;
    load_tile(xu, xoff, xin0);
    load_tile(xu + IMGROWB, xoff, xin1);
  }
#pragma unroll
  for (int i = 0; i < 4 * WMAT_BYTES / 16 / 512; ++i)
    lds_st(smem + LDS_W + (i * 512 + tid) * 16, *(const u32x4*)(wf + (i * 512 + tid) * 16));
  if (tid < 16) ((lds_u32*)(smem + LDS_CNT))[tid] = 0u;
  __syncthreads();

  lds_u32* cntW = (lds_u32*)(smem + LDS_CNT) + grp * 8;
  lds_u32* cntR = cntW + 4;
  const lds_char* wl = smem + LDS_W + lane * 16;
  lds_char* xg = smem + LDS_X + grp * 4 * XREG;
  lds_char* xw = xg + f * XREG + (hh * 32 + 8 * wid + p) * 16;
  lds_char* xr = xg + wid * XREG;
  const f32x16 zero = {};
  TMasks mk;
  {
    mk.s0 = ((p >> 1) & 1) ? ~0u : 0u;
    mk.s1 = ((p >> 2) & 1) ? ~0u : 0u;
    const bool valid = (hh == (p & 1));
#pragma unroll
    for (int g = 0; g < 4; ++g) { mk.g[g] = (valid && (p >> 1) == g) ? ~0u : 0u; asm("" : "+v"(mk.g[g])); }
    asm("" : "+v"(mk.s0)); asm("" : "+v"(mk.s1));
  }
  const unsigned ost_w = (r >> 3) * OROW + (r & 7) * OTOK + hh * 16;
  unsigned ost_r[3];
#pragma unroll
  for (int i = 0; i < 3; ++i) { int n = 16 * i + (lane & 15); ost_r[i] = (lane >> 4) * OROW + (n + n / 12) * 16; }
  const unsigned ooff = (lane >> 4) * 2 * IMGROWB + (lane & 15) * 16;

  unsigned ph = 0;
  for (;;) {
    const int wy = unit / NWIN, wx = unit % NWIN;
    STAMP(1);
    u32x4 x1[2][6], h2[2][6];
    {
      f32x16 xs[3]; u32x4 hf[6], pf[2], o8[6];
      layernorm_tile(xin0, xs, hf);
      STAMP(2);
      temporal_scores(wl, hf, mk, pf);
      STAMP(3);
      temporal_out(wl, hf, pf, xs, o8);
      STAMP(4);
      grp_wait(cntR, ph);
#pragma unroll
      for (int s = 0; s < 6; ++s) lds_st(xw + s * 1024, o8[s]);
      grp_arrive(cntW, lane);
    }
    STAMP(5);
    {
      f32x16 xs[3]; u32x4 hf[6], pf[2], o8[6];
      layernorm_tile(xin1, xs, hf);
      temporal_scores(wl, hf, mk, pf);
      grp_wait(cntW, ph + 4);
#pragma unroll
      for (int s = 0; s < 6; ++s) x1[0][s] = lds_ld(xr + s * 1024 + lane * 16);
      grp_arrive(cntR, lane);
      temporal_out(wl, hf, pf, xs, o8);
      grp_wait(cntR, ph + 4);
#pragma unroll
      for (int s = 0; s < 6; ++s) lds_st(xw + s * 1024, o8[s]);
      grp_arrive(cntW, lane);
    }
    STAMP(6);
    { f32x16 xs[3]; layernorm_tile(x1[0], xs, h2[0]); }
    STAMP(7);
    grp_wait(cntW, ph + 8);
    STAMP(8);
#pragma unroll
    for (int s = 0; s < 6; ++s) x1[1][s] = lds_ld(xr + s * 1024 + lane * 16);
    { f32x16 xs[3]; layernorm_tile(x1[1], xs, h2[1]); }
#pragma unroll
    for (int q = 0; q < 2; ++q)
#pragma unroll
      for (int s = 0; s < 6; ++s) asm("" : "+v"(x1[q][s]));
    STAMP(9);
    u32x4 pf[2][4];
#pragma unroll
    for (int q = 0; q < 2; ++q) {
      asm volatile("" ::: "memory");
      f32x16 sa0 = zero, sa1 = zero;
#pragma unroll
      for (int T = 0; T < 3; ++T) {
        f32x16 g = zero;
#pragma unroll
        for (int s = 0; s < 6; ++s) g = MFMA(asfrag(lds_ld(wl + 2 * WMAT_BYTES + (T * 6 + s) * 1024)), asfrag(h2[q][s]), g);
        u32x4 g0 = pack8(g, 0), g1 = pack8(g, 1);
        sa0 = MFMA(asfrag(h2[0][2 * T]), asfrag(g0), sa0);
        sa1 = MFMA(asfrag(h2[1][2 * T]), asfrag(g0), sa1);
        sa0 = MFMA(asfrag(h2[0][2 * T + 1]), asfrag(g1), sa0);
        sa1 = MFMA(asfrag(h2[1][2 * T + 1]), asfrag(g1), sa1);
      }
      float m0 = __builtin_fmaxf(sa0[0], sa1[0]), m1 = __builtin_fmaxf(sa0[1], sa1[1]);
#pragma unroll
      for (int k = 2; k < 16; k += 2) {
        m0 = __builtin_fmaxf(__builtin_fmaxf(m0, sa0[k]), sa1[k]);
        m1 = __builtin_fmaxf(__builtin_fmaxf(m1, sa0[k + 1]), sa1[k + 1]);
      }
      float mx = xhalf_max(__builtin_fmaxf(m0, m1));
      float l0 = 0.f, l1 = 0.f;
#pragma unroll
      for (int k = 0; k < 16; ++k) {
        sa0[k] = __builtin_amdgcn_exp2f(sa0[k] - mx); l0 += sa0[k];
        sa1[k] = __builtin_amdgcn_exp2f(sa1[k] - mx); l1 += sa1[k];
      }
      float rinv = __builtin_amdgcn_rcpf(xhalf_sum(l0 + l1));
#pragma unroll
      for (int k = 0; k < 16; ++k) { sa0[k] *= rinv; sa1[k] *= rinv; }
      pf[q][0] = pack8(sa0, 0); pf[q][1] = pack8(sa0, 1);
      pf[q][2] = pack8(sa1, 0); pf[q][3] = pack8(sa1, 1);
    }
    STAMP(10);
#pragma unroll
    for (int T = 0; T < 3; ++T) {
      f32x16 v0 = zero, v1 = zero;
#pragma unroll
      for (int s = 0; s < 6; ++s) {
        bf16x8 w = asfrag(lds_ld(wl + 3 * WMAT_BYTES + (T * 6 + s) * 1024));
        v0 = MFMA(asfrag(h2[0][s]), w, v0);
        v1 = MFMA(asfrag(h2[1][s]), w, v1);
      }
      u32x4 vf[4] = {pack8(v0, 0), pack8(v0, 1), pack8(v1, 0), pack8(v1, 1)};
#pragma unroll
      for (int q = 0; q < 2; ++q) {
        f32x16 o = unpack16(x1[q][2 * T], x1[q][2 * T + 1]);
#pragma unroll
        for (int k = 0; k < 4; ++k) o = MFMA(asfrag(vf[k]), asfrag(pf[q][k]), o);
        x1[q][2 * T] = pack8(o, 0);
        x1[q][2 * T + 1] = pack8(o, 1);
      }
    }
    STAMP(11);
    const int nunit = unit + 2 * NWG;
    const bool more = nunit < NUNITS;
    if (more) {
      const int ny = nunit / NWIN, nx = nunit % NWIN;
      const char* xu = x + (size_t)(ny * 8 + 2 * wid) * IMGROWB + (size_t)(nx * 8) * ROWB;
      load_tile(xu, xoff, xin0);
      load_tile(xu + IMGROWB, xoff, xin1);
    } else {
#pragma unroll
      for (int s = 0; s < 6; ++s) { xin0[s] = u32x4{}; xin1[s] = u32x4{}; }
    }
    char* ou = out + (size_t)wid * FRAMEB + (size_t)(wy * 8) * IMGROWB + (size_t)(wx * 8) * ROWB;
#pragma unroll
    for (int q = 0; q < 2; ++q) {
#pragma unroll
      for (int s = 0; s < 6; ++s) lds_st(xr + ost_w + 32 * s, x1[q][s]);
      asm volatile("s_waitcnt lgkmcnt(0)" ::: "memory");
      u32x4 v[6];
#pragma unroll
      for (int i = 0; i < 6; ++i) v[i] = lds_ld(xr + ost_r[i % 3] + (i / 3) * 4 * OTOK);
#pragma unroll
      for (int i = 0; i < 6; ++i) *(u32x4*)(ou + (size_t)q * IMGROWB + ooff + 256 * i) = v[i];
      asm volatile("s_waitcnt lgkmcnt(0)" ::: "memory");
    }
    STAMP(12);
    if (!more) break;
    grp_arrive(cntR, lane);
    ph += 8;
    unit = nunit;
  }
}

extern "C" void kernel_launch(void* const* d_in, const int* in_sizes, int n_in, void* d_out, int out_size,
                              void* d_ws, size_t ws_size, hipStream_t stream) {
  const __hip_bfloat16* x = (const __hip_bfloat16*)d_in[0];
  const __hip_bfloat16* wqkv_t = (const __hip_bfloat16*)d_in[1];
  const __hip_bfloat16* wo_t = (const __hip_bfloat16*)d_in[2];
  const __hip_bfloat16* wqkv_s = (const __hip_bfloat16*)d_in[3];
  const __hip_bfloat16* wo_s = (const __hip_bfloat16*)d_in[4];
  __hip_bfloat16* wf = (__hip_bfloat16*)d_ws;
  fold_kernel<<<(4 * 9216 + 255) / 256, 256, 0, stream>>>(wqkv_t, wo_t, wqkv_s, wo_s, wf);
  (void)hipFuncSetAttribute((const void*)swin_kernel, hipFuncAttributeMaxDynamicSharedMemorySize, LDS_TOTAL);
  swin_kernel<<<NWG, 512, LDS_TOTAL, stream>>>((const char*)x, (const char*)wf, (char*)d_out);
}
```

```cpp
#include <hip/hip_runtime.h>
#include <hip/hip_bf16.h>
#include <stdint.h>

using bf16x8 = __attribute__((ext_vector_type(8))) short;
using f32x16 = __attribute__((ext_vector_type(16))) float;
using u32x4 = __attribute__((ext_vector_type(4))) unsigned;
using f32x2 = __attribute__((ext_vector_type(2))) float;
using bf2 = __attribute__((ext_vector_type(2))) __bf16;
#if defined(__HIP_DEVICE_COMPILE__)
#define NOPK __attribute__((target("no-packed-fp32-ops")))
#else
#define NOPK
#endif
#define DI __device__ __forceinline__ NOPK
#define MFMA(a, b, c) __builtin_amdgcn_mfma_f32_32x32x16_bf16(__builtin_bit_cast(bf16x8, (a)), __builtin_bit_cast(bf16x8, (b)), (c), 0, 0, 0)
#define SB() __builtin_amdgcn_sched_barrier(0)
#define PIN(x) asm volatile("" : "+v"(x))
#define STORE_AUX 16

constexpr int CH = 96, HH = 224, WW = 224;
constexpr int ROWB = CH * 2;
constexpr int FRAMEB = HH * WW * ROWB;
constexpr int IMGROWB = WW * ROWB;
constexpr int NWIN = 28;
constexpr int NUNITS = NWIN * NWIN;
constexpr int WMAT_BYTES = 18 * 1024;
constexpr float LN_EPS = 1e-5f;
constexpr int NWG = 256;
constexpr float QK_SCALE = 0.10206207261596575f * 1.4426950408889634f;

template <int I> struct IC { static constexpr int v = I; };
template <int B, int E, class F> DI void sfor(F&& f) { if constexpr (B < E) { f(IC<B>{}); sfor<B + 1, E>(f); } }
#define LAM(M) [&](auto M) __attribute__((always_inline)) NOPK

DI unsigned pk2(float a, float b) {
  f32x2 v = {a, b};
  return __builtin_bit_cast(unsigned, __builtin_convertvector(v, bf2));
}
DI float lo16(unsigned u) { return __builtin_bit_cast(float, u << 16); }
DI float hi16(unsigned u) { return __builtin_bit_cast(float, u & 0xffff0000u); }
DI unsigned fbits(float f) { return __builtin_bit_cast(unsigned, f); }
DI float bitsf(unsigned u) { return __builtin_bit_cast(float, u); }
DI float xhalf_sum(float v) {
  auto r = __builtin_amdgcn_permlane32_swap(fbits(v), fbits(v), false, false);
  return bitsf(r[0]) + bitsf(r[1]);
}
DI float xhalf_max(float v) {
  auto r = __builtin_amdgcn_permlane32_swap(fbits(v), fbits(v), false, false);
  return __builtin_fmaxf(bitsf(r[0]), bitsf(r[1]));
}
DI float bsel(unsigned m, float a, float b) { return bitsf((m & fbits(a)) | (~m & fbits(b))); }
DI float max3(float a, float b, float c) { return __builtin_fmaxf(__builtin_fmaxf(a, b), c); }
DI u32x4 pack8(const f32x16& a, int s) {
  u32x4 r;
  r[0] = pk2(a[8 * s + 0], a[8 * s + 1]);
  r[1] = pk2(a[8 * s + 2], a[8 * s + 3]);
  r[2] = pk2(a[8 * s + 4], a[8 * s + 5]);
  r[3] = pk2(a[8 * s + 6], a[8 * s + 7]);
  return r;
}

typedef __attribute__((address_space(3))) char lds_char;
typedef __attribute__((address_space(3))) unsigned lds_u32;
DI u32x4 lds_ld(const lds_char* p) { return *(const __attribute__((address_space(3))) u32x4*)p; }
DI void lds_st(lds_char* p, u32x4 v) { *(__attribute__((address_space(3))) u32x4*)p = v; }

constexpr int LDS_CNT = 0;
DI void cnt_arrive(const lds_char* l16, unsigned dvoff, int k) {
  asm volatile("s_lshl_b64 exec, 1, %2\n\tds_add_u32 %0, %1 offset:%3\n\ts_mov_b64 exec, -1"
               :: "v"((unsigned)(size_t)l16), "v"(dvoff), "s"(k), "i"(LDS_CNT) : "memory", "scc");
}
DI unsigned cnt_peek(const lds_char* l16) {
  return __hip_atomic_load((const lds_u32*)(l16 + LDS_CNT), __ATOMIC_RELAXED, __HIP_MEMORY_SCOPE_WORKGROUP);
}
DI void cnt_wait(const lds_char* l16, int k, unsigned n, unsigned peeked) {
  const unsigned target = n * 16u * (unsigned)k;
  asm volatile("" ::: "memory");
  if (__builtin_expect((unsigned)__builtin_amdgcn_readlane(peeked, k) < target, 0)) {
    for (;;) {
      __builtin_amdgcn_s_sleep(1);
      if ((unsigned)__builtin_amdgcn_readlane(cnt_peek(l16), k) >= target) break;
    }
  }
  asm volatile("" ::: "memory");
}
DI void cnt_wait(const lds_char* l16, int k, unsigned n) { cnt_wait(l16, k, n, cnt_peek(l16)); }

typedef __attribute__((address_space(1))) unsigned gu32;
constexpr int SCR_OFF = 65536;
constexpr int XFS = 1552;
constexpr int XREG = 6304;
constexpr int OSTG = 4096;
constexpr int LDS_ID = 256;
constexpr int LDS_W0 = LDS_ID + 2048;
constexpr int LDS_X = LDS_W0 + 2 * WMAT_BYTES;
constexpr int LDS_W2 = LDS_X + 8 * XREG;
constexpr int LDS_O = LDS_W2 + 2 * WMAT_BYTES;
constexpr int LDS_RAW = LDS_W2;
constexpr int LDS_RAW_END = LDS_RAW + 73728;
constexpr int LDS_TOTAL = LDS_RAW_END;
static_assert(LDS_O + 8 * OSTG <= LDS_RAW_END && LDS_O + 2 * WMAT_BYTES <= LDS_RAW_END, "staging");
static_assert(LDS_TOTAL <= 163840, "lds");

DI void dma_tile(const char* row, unsigned voff, unsigned lds) {
  const char* r1 = row + FRAMEB; const char* r2 = row + 2 * (size_t)FRAMEB; const char* r3 = row + 3 * (size_t)FRAMEB;
  unsigned keep;
  asm volatile(
      "s_mov_b32 %0, m0\n\t"
      "s_mov_b32 m0, %6\n\ts_nop 0\n\t"
      "global_load_lds_dwordx4 %1, %2 nt\n\t"
      "s_add_u32 m0, m0, %7\n\ts_nop 0\n\t"
      "global_load_lds_dwordx4 %1, %3 nt\n\t"
      "s_add_u32 m0, m0, %7\n\ts_nop 0\n\t"
      "global_load_lds_dwordx4 %1, %4 nt\n\t"
      "s_add_u32 m0, m0, %7\n\ts_nop 0\n\t"
      "global_load_lds_dwordx4 %1, %5 nt\n\t"
      "s_mov_b32 exec_hi, 0\n\t"
      "global_load_lds_dwordx4 %1, %5 offset:1024 nt\n\t"
      "s_sub_u32 m0, m0, %7\n\ts_nop 0\n\t"
      "global_load_lds_dwordx4 %1, %4 offset:1024 nt\n\t"
      "s_sub_u32 m0, m0, %7\n\ts_nop 0\n\t"
      "global_load_lds_dwordx4 %1, %3 offset:1024 nt\n\t"
      "s_sub_u32 m0, m0, %7\n\ts_nop 0\n\t"
      "global_load_lds_dwordx4 %1, %2 offset:1024 nt\n\t"
      "s_mov_b32 exec_hi, -1\n\t"
      "s_mov_b32 m0, %0"
      : "=&s"(keep) : "v"(voff), "s"(row), "s"(r1), "s"(r2), "s"(r3), "s"(lds), "i"(XFS) : "memory", "scc");
}
DI void dma_tile_sc1(const char* row, unsigned voff, unsigned lds) {
  const char* r1 = row + FRAMEB; const char* r2 = row + 2 * (size_t)FRAMEB; const char* r3 = row + 3 * (size_t)FRAMEB;
  unsigned keep;
  asm volatile(
      "s_mov_b32 %0, m0\n\t"
      "s_mov_b32 m0, %6\n\ts_nop 0\n\t"
      "global_load_lds_dwordx4 %1, %2 sc1\n\t"
      "s_add_u32 m0, m0, %7\n\ts_nop 0\n\t"
      "global_load_lds_dwordx4 %1, %3 sc1\n\t"
      "s_add_u32 m0, m0, %7\n\ts_nop 0\n\t"
      "global_load_lds_dwordx4 %1, %4 sc1\n\t"
      "s_add_u32 m0, m0, %7\n\ts_nop 0\n\t"
      "global_load_lds_dwordx4 %1, %5 sc1\n\t"
      "s_mov_b32 exec_hi, 0\n\t"
      "global_load_lds_dwordx4 %1, %5 offset:1024 sc1\n\t"
      "s_sub_u32 m0, m0, %7\n\ts_nop 0\n\t"
      "global_load_lds_dwordx4 %1, %4 offset:1024 sc1\n\t"
      "s_sub_u32 m0, m0, %7\n\ts_nop 0\n\t"
      "global_load_lds_dwordx4 %1, %3 offset:1024 sc1\n\t"
      "s_sub_u32 m0, m0, %7\n\ts_nop 0\n\t"
      "global_load_lds_dwordx4 %1, %2 offset:1024 sc1\n\t"
      "s_mov_b32 exec_hi, -1\n\t"
      "s_mov_b32 m0, %0"
      : "=&s"(keep) : "v"(voff), "s"(row), "s"(r1), "s"(r2), "s"(r3), "s"(lds), "i"(XFS) : "memory", "scc");
}
DI void dma_piece(const char* base, unsigned voff, unsigned lds) {
  unsigned keep;
  asm volatile("s_mov_b32 %0, m0\n\ts_mov_b32 m0, %3\n\ts_nop 0\n\tglobal_load_lds_dwordx4 %1, %2\n\ts_mov_b32 m0, %0"
               : "=&s"(keep) : "v"(voff), "s"(base), "s"(lds) : "memory");
}
#define VMWAIT(n) asm volatile("s_waitcnt vmcnt(" #n ")" ::: "memory")
#define LGWAIT() asm volatile("s_waitcnt lgkmcnt(0)" ::: "memory")

#define STAMP(i) do {} while (0)
#define PSTAMP(i) do {} while (0)
#define QSTAMP(i) do {} while (0)

template <int CPR, int SS>
DI void raw_piece(const char* src, int piece, int lane, unsigned lds_img) {
  const unsigned q = (unsigned)(piece * 64 + lane);
  const unsigned row = (q * (unsigned)(((1 << 20) + CPR - 1) / CPR)) >> 20;
  const unsigned off = (SS == CPR * 16) ? 16u * (q ^ ((row >> 2) & 3u)) : row * (unsigned)(SS - CPR * 16) + 16u * (q ^ ((row >> 2) & 3u));
  dma_piece(src, off, lds_img + piece * 1024);
}
typedef short s16x4 __attribute__((ext_vector_type(4)));
DI void fold_read_rows(u32x4* d, const lds_char* img, int rb, int row0, int sw, int hh) {
  const int x = (sw >> 2) & 3;
  const lds_char* b0 = img + (row0 + sw) * rb + 16 * (hh ^ x);
  const lds_char* b1 = img + (row0 + sw) * rb + 16 * ((2 + hh) ^ x);
#pragma unroll
  for (int s = 0; s < 6; ++s) d[s] = lds_ld(((s & 1) ? b1 : b0) + 64 * (s >> 1));
}
DI void fold_read_cols(u32x4* d, const lds_char* img, int T, int lane) {
  const int hh = lane >> 5, cb = (lane >> 4) & 1, qq = (lane >> 2) & 3, pp = lane & 3;
  const int cc = 4 * T + 2 * cb + (pp & 1);
  const lds_char* b0 = img + (8 * hh + qq) * 192 + 16 * (cc ^ ((2 * hh) & 3)) + 8 * (pp >> 1);
  const lds_char* b1 = img + (8 * hh + qq + 4) * 192 + 16 * (cc ^ ((2 * hh + 1) & 3)) + 8 * (pp >> 1);
#pragma unroll
  for (int s = 0; s < 6; ++s) {
    s16x4 lo = __builtin_amdgcn_ds_read_tr16_b64_v4i16((__attribute__((address_space(3))) s16x4*)(b0 + s * 16 * 192));
    s16x4 hi = __builtin_amdgcn_ds_read_tr16_b64_v4i16((__attribute__((address_space(3))) s16x4*)(b1 + s * 16 * 192));
    d[s] = __builtin_bit_cast(u32x4, __builtin_shufflevector(lo, hi, 0, 1, 2, 3, 4, 5, 6, 7));
  }
}
DI void fold_compute(const u32x4 (&a)[18], const u32x4 (&b)[6], float sc, u32x4 (&res)[6]) {
  const f32x16 zero = {};
  f32x16 acc[3];
#pragma unroll
  for (int s = 0; s < 6; ++s)
#pragma unroll
    for (int Tin = 0; Tin < 3; ++Tin) acc[Tin] = MFMA(a[Tin * 6 + s], b[s], s == 0 ? zero : acc[Tin]);
#pragma unroll
  for (int Tin = 0; Tin < 3; ++Tin) {
#pragma unroll
    for (int i = 0; i < 16; ++i) acc[Tin][i] *= sc;
    res[2 * Tin] = pack8(acc[Tin], 0);
    res[2 * Tin + 1] = pack8(acc[Tin], 1);
  }
}
DI void fold_store(lds_char* wdst, const u32x4 (&res)[6]) {
#pragma unroll
  for (int s = 0; s < 6; ++s) lds_st(wdst + s * 1024, res[s]);
}

struct Stats { float s0, s1, q0, q1, rs, nb; };
DI void stat_pkt(const u32x4 (&xf)[6], f32x16 (&xs)[3], Stats& st, int k) {
  const int s = k >> 2, i = k & 3, T = s >> 1, e = 8 * (s & 1) + 2 * i;
  const float a = lo16(xf[s][i]), b = hi16(xf[s][i]);
  xs[T][e] = a; xs[T][e + 1] = b;
  if (k == 0) { st.s0 = a; st.s1 = b; st.q0 = a * a; st.q1 = b * b; }
  else { st.s0 += a; st.s1 += b; st.q0 = __builtin_fmaf(a, a, st.q0); st.q1 = __builtin_fmaf(b, b, st.q1); }
  PIN(st.s0); PIN(st.s1); PIN(st.q0); PIN(st.q1);
}
DI void stat_fin(Stats& st) {
  float s = xhalf_sum(st.s0 + st.s1), q = xhalf_sum(st.q0 + st.q1);
  float m = s * (1.f / CH);
  float var = __builtin_fmaxf(__builtin_fmaf(-m, m, q * (1.f / CH)), 0.f);
  st.rs = __builtin_amdgcn_rsqf(var + LN_EPS);
  st.nb = -m * st.rs;
  PIN(st.rs); PIN(st.nb);
}
DI void nrm_pkt(const f32x16 (&xs)[3], u32x4 (&hf)[6], const Stats& st, int k) {
  const int s = k >> 2, i = k & 3, T = s >> 1, e = 8 * (s & 1) + 2 * i;
  unsigned v = pk2(__builtin_fmaf(xs[T][e], st.rs, st.nb), __builtin_fmaf(xs[T][e + 1], st.rs, st.nb));
  PIN(v);
  hf[s][i] = v;
}
DI void cvt4(u32x4& d, const f32x16& a, int s) { d = pack8(a, s); PIN(d); }

constexpr int RD = 4;
constexpr int NWSEQ = 144;
DI constexpr int woff(int n) {
  n %= NWSEQ;
  if (n < 72) {
    int k = n % 36;
    if (k < 12) return LDS_W0 + (k & 1) * WMAT_BYTES + (k >> 1) * 1024;
    if (k < 24) return LDS_W0 + (6 + (k - 12)) * 1024;
    return LDS_W0 + WMAT_BYTES + (6 + (k - 24)) * 1024;
  }
  if (n < 108) return LDS_W2 + ((n - 72) % 18) * 1024;
  return LDS_W2 + WMAT_BYTES + (((n - 108) / 12) * 6 + (n - 108) % 6) * 1024;
}
constexpr int WB_BASE = LDS_W2;
struct Ctx {
  const lds_char* wa;
  const lds_char* wb;
  bool ms0, ms1, mg[4];
};
DI u32x4 wload(const Ctx& c, int n) {
  const int o = woff(n);
  return o < LDS_W2 ? lds_ld(c.wa + o) : lds_ld(c.wb + (o - WB_BASE));
}
#define WUSE(n) ring[(n) % RD]
#define WNEXT(n) ring[(n) % RD] = wload(c, (n) + RD)

struct Slot { int ty, T, s; };
DI constexpr Slot tslot(int m) {
  if (m < 12) return {m & 1, 0, m >> 1};
  if (m < 15) return {0, 1, m - 12};
  if (m < 17) return {2, 0, m - 15};
  if (m < 20) return {0, 1, m - 14};
  if (m < 23) return {0, 2, m - 20};
  if (m < 25) return {2, 1, m - 23};
  if (m < 28) return {0, 2, m - 22};
  if (m < 31) return {1, 1, m - 28};
  if (m < 33) return {2, 2, m - 31};
  if (m < 36) return {1, 1, m - 30};
  if (m < 42) return {1, 2, m - 36};
  return {3, (m - 42) >> 1, (m - 42) & 1};
}
DI constexpr int twidx(int m) {
  return m < 15 ? m : (m < 17 ? 15 : (m < 23 ? m - 2 : (m < 25 ? 21 : (m < 31 ? m - 4 : (m < 33 ? 27 : (m < 42 ? m - 6 : 36))))));
}
DI constexpr int sched_cnt(int m, int first, int last, int total) {
  return m <= first ? 0 : (m >= last ? total : (m - first) * total / (last - first));
}

struct TState { f32x16 g[3], v[3], sa; u32x4 hf[6], gp[2], vp[3][2], pf[2]; float e[4]; unsigned pk0, pk1; };
template <int K0, int K1>
DI void stat_pkts(const u32x4 (&xf)[6], f32x16 (&xs)[3], Stats& st) {
  if constexpr (K0 < K1) { stat_pkt(xf, xs, st, K0); stat_pkts<K0 + 1, K1>(xf, xs, st); }
}
template <int N0, int m, int M1, int HS, bool PE, bool OE>
DI void temporal_slots(const Ctx& c, u32x4 (&ring)[RD], TState& t, f32x16 (&xs)[3], const Stats& st,
                       const u32x4 (&nx)[6], f32x16 (&nxs)[3], Stats& nst, u32x4 (&nhf)[6], const f32x16 (&pxs)[3], lds_char* xw) {
  const f32x16 zero = {};
  if constexpr (m < M1) {
    constexpr Slot sl = tslot(m);
    constexpr int n = N0 + twidx(m);
    if constexpr (sl.ty == 0) { t.g[sl.T] = MFMA(WUSE(n), t.hf[sl.s], sl.s == 0 ? zero : t.g[sl.T]); WNEXT(n); }
    else if constexpr (sl.ty == 1) { t.v[sl.T] = MFMA(t.hf[sl.s], WUSE(n), sl.s == 0 ? zero : t.v[sl.T]); WNEXT(n); }
    else if constexpr (sl.ty == 2) t.sa = MFMA(t.hf[2 * sl.T + sl.s], t.gp[sl.s], (sl.T == 0 && sl.s == 0) ? zero : t.sa);
    else xs[sl.T] = MFMA(t.vp[sl.T][sl.s], t.pf[sl.s], xs[sl.T]);
    SB();
    if constexpr (m < 10) { nrm_pkt(xs, t.hf, st, 4 + 2 * m); nrm_pkt(xs, t.hf, st, 5 + 2 * m); }
    if constexpr (PE && m >= 2 && m < 8) { u32x4 a; cvt4(a, pxs[(m - 2) >> 1], m & 1); lds_st(xw + (m - 2) * 1024, a); }
    if constexpr (m == 12) cvt4(t.gp[0], t.g[0], 0);
    if constexpr (m == 13) cvt4(t.gp[1], t.g[0], 1);
    if constexpr (m == 14) cvt4(t.vp[0][0], t.v[0], 0);
    if constexpr (m == 15) cvt4(t.vp[0][1], t.v[0], 1);
    if constexpr (m == 21) { cvt4(t.gp[0], t.g[1], 0); }
    if constexpr (m == 22) { cvt4(t.gp[1], t.g[1], 1); }
    if constexpr (m == 29) { cvt4(t.gp[0], t.g[2], 0); }
    if constexpr (m == 30) { cvt4(t.gp[1], t.g[2], 1); }
    if constexpr (m == 35 || m == 36) {
#pragma unroll
      for (int k = 2 * (m - 35); k < 2 * (m - 35) + 2; ++k) {
        float s1 = t.sa[4 + k], s3 = t.sa[12 + k];
        asm("" : "+v"(s1)); asm("" : "+v"(s3));
        float a = c.ms0 ? s1 : t.sa[k];
        float b = c.ms0 ? s3 : t.sa[8 + k];
        t.e[k] = c.ms1 ? b : a; PIN(t.e[k]);
      }
    }
    if constexpr (m == 37) {
      float mx = max3(max3(t.e[0], t.e[1], t.e[2]), t.e[3], t.e[3]);
#pragma unroll
      for (int k = 0; k < 4; ++k) { t.e[k] = __builtin_amdgcn_exp2f(t.e[k] - mx); PIN(t.e[k]); }
    }
    if constexpr (m == 38) {
      float rinv = __builtin_amdgcn_rcpf((t.e[0] + t.e[1]) + (t.e[2] + t.e[3]));
      t.pk0 = pk2(t.e[0] * rinv, t.e[1] * rinv); t.pk1 = pk2(t.e[2] * rinv, t.e[3] * rinv);
      PIN(t.pk0); PIN(t.pk1);
    }
    if constexpr (m == 39) {
#pragma unroll
      for (int g = 0; g < 4; ++g) {
        t.pf[g >> 1][(g & 1) * 2 + 0] = c.mg[g] ? t.pk0 : 0u;
        t.pf[g >> 1][(g & 1) * 2 + 1] = c.mg[g] ? t.pk1 : 0u;
      }
      PIN(t.pf[0]); PIN(t.pf[1]);
    }
    if constexpr (m == 40) { cvt4(t.vp[1][0], t.v[1], 0); cvt4(t.vp[1][1], t.v[1], 1); }
    if constexpr (m == 44) { cvt4(t.vp[2][0], t.v[2], 0); cvt4(t.vp[2][1], t.v[2], 1); }
    if constexpr (m < 35) stat_pkts<sched_cnt(m, HS + 2, 35, 20), sched_cnt(m + 1, HS + 2, 35, 20)>(nx, nxs, nst);
    if constexpr (m == 41) stat_pkts<20, 22>(nx, nxs, nst);
    if constexpr (m == 42) stat_pkts<22, 24>(nx, nxs, nst);
    if constexpr (m == 43) stat_fin(nst);
    if constexpr (m == 45) { nrm_pkt(nxs, nhf, nst, 0); nrm_pkt(nxs, nhf, nst, 1); }
    if constexpr (m == 46) { nrm_pkt(nxs, nhf, nst, 2); nrm_pkt(nxs, nhf, nst, 3); }
    if constexpr (OE && m == 46) { u32x4 a; cvt4(a, xs[0], 0); lds_st(xw, a); }
    if constexpr (OE && m == 47) { u32x4 a; cvt4(a, xs[0], 1); lds_st(xw + 1024, a); }
    SB();
    temporal_slots<N0, m + 1, M1, HS, PE, OE>(c, ring, t, xs, st, nx, nxs, nst, nhf, pxs, xw);
  }
}

struct SMState { float m0, m1, mx, l0, l1, rinv; };
template <int u>
DI void sm_unit(f32x16& a, f32x16& b, SMState& s, u32x4 (&pf)[4]) {
  if constexpr (u < 4) {
    f32x16& x = (u < 2) ? a : b;
    const int o = 8 * (u & 1);
    if (u == 0) { s.m0 = max3(x[0], x[1], x[2]); s.m1 = max3(x[3], x[4], x[5]); s.m0 = max3(s.m0, x[6], x[7]); }
    else { s.m0 = max3(s.m0, x[o], x[o + 1]); s.m1 = max3(s.m1, x[o + 2], x[o + 3]); s.m0 = max3(s.m0, x[o + 4], x[o + 5]); s.m1 = max3(s.m1, x[o + 6], x[o + 7]); }
    PIN(s.m0); PIN(s.m1);
  } else if constexpr (u == 4) {
    s.mx = xhalf_max(__builtin_fmaxf(s.m0, s.m1)); PIN(s.mx);
  } else if constexpr (u < 21) {
    constexpr int k = u - 5;
    f32x16& x = (k < 8) ? a : b;
    constexpr int o = 2 * (k & 7);
    float e0 = __builtin_amdgcn_exp2f(x[o] - s.mx), e1 = __builtin_amdgcn_exp2f(x[o + 1] - s.mx);
    PIN(e0); PIN(e1);
    x[o] = e0; x[o + 1] = e1;
    if constexpr (k >= 2) {
      f32x16& y = (k - 1 < 8) ? a : b;
      constexpr int o1 = 2 * ((k - 1) & 7);
      if constexpr (k == 2) { s.l0 = a[0] + y[o1]; s.l1 = a[1] + y[o1 + 1]; } else { s.l0 += y[o1]; s.l1 += y[o1 + 1]; }
      PIN(s.l0); PIN(s.l1);
    }
  } else if constexpr (u == 21) {
    s.rinv = __builtin_amdgcn_rcpf(xhalf_sum((s.l0 + b[14]) + (s.l1 + b[15]))); PIN(s.rinv);
  } else {
    const int k = u - 22;
    f32x16& x = (k < 4) ? a : b;
    const int o = 4 * (k & 3);
    unsigned p0 = pk2(x[o] * s.rinv, x[o + 1] * s.rinv), p1 = pk2(x[o + 2] * s.rinv, x[o + 3] * s.rinv);
    PIN(p0); PIN(p1);
    pf[k >> 1][2 * (k & 1)] = p0; pf[k >> 1][2 * (k & 1) + 1] = p1;
  }
}

__global__ __launch_bounds__(512, 2) NOPK void swin_kernel(const char* __restrict__ x, const char* __restrict__ wqkv_t,
                                                           const char* __restrict__ wo_t, const char* __restrict__ wqkv_s,
                                                           const char* __restrict__ wo_s, char* __restrict__ out,
                                                           char* __restrict__ ws) {
  extern __shared__ __attribute__((aligned(16))) char smem_[];
  lds_char* smem = (lds_char*)smem_;
  const unsigned smem_a = (unsigned)(size_t)smem;
  const int tid = threadIdx.x, lane = tid & 63;
  const int wid8 = __builtin_amdgcn_readfirstlane(tid >> 6);
  const int grp = wid8 >> 2, wid = wid8 & 3;
  const int r = lane & 31, hh = lane >> 5;
  const int p = r >> 2, f = r & 3;
  const int sw = (r & 0x13) | ((r & 4) << 1) | ((r & 8) >> 1);
  const unsigned dvoff = lane * 16;
  const unsigned xr_a = smem_a + LDS_X + wid8 * XREG;

  constexpr int FULL = 0, TONLY = 1, SONLY = 2, NONE = 3;
  const int bid = blockIdx.x;
  int mode = FULL, unit = bid + NWG * grp, nmode = grp == 0 ? FULL : NONE, nunit = bid + 2 * NWG;
  if (grp == 0 && bid < 16) { mode = TONLY; unit = 3 * NWG + bid; nunit = bid; }
  const int lastmode = (grp == 0 && bid >= 16 && bid < 32) ? SONLY : NONE;
  STAMP(0);
  gu32* hfl = (gu32*)(ws + ((bid & 15) * 4 + wid) * 256);
  unsigned hev;
  asm volatile("global_load_dword %0, %1, %2 sc1" : "=v"(hev) : "v"(0u), "s"(hfl) : "memory");
  const lds_char* l16 = smem + lane * 16;
  constexpr int C_TW = 5, C_MS = 6, C_RAW = 7, C_VS = 8, C_QK = 9;
  constexpr int IMG = WMAT_BYTES;
  {
    const int rot = (blockIdx.x * 11) % 72;
#pragma unroll
    for (int i = 0; i < 9; ++i) {
      int pc = wid8 * 9 + i + rot; pc -= pc >= 72 ? 72 : 0;
      if (pc < 54) raw_piece<36, 576>(wqkv_t, pc, lane, smem_a + LDS_RAW);
      else raw_piece<12, 192>(wo_t, pc - 54, lane, smem_a + LDS_RAW + 3 * IMG);
    }
#pragma unroll
    for (int i = 0; i < 5; ++i) {
      int pc = (wid8 * 5 + i + rot) % 36;
      raw_piece<24, 576>(wqkv_s, pc, lane, smem_a + LDS_W0);
    }
    if (tid < 64) ((lds_u32*)(smem + LDS_CNT))[tid] = 0u;
    asm volatile("s_barrier" ::: "memory");
    {
      const int wy = unit / NWIN, wx = unit % NWIN;
      dma_tile(x + (size_t)(wy * 8 + 2 * wid) * IMGROWB + (size_t)(wx * 8) * ROWB, dvoff, xr_a);
    }
    PSTAMP(0);
    if (wid8 == 7) {
      u32x4 idf[2];
#pragma unroll
      for (int s = 0; s < 2; ++s)
#pragma unroll
        for (int d = 0; d < 4; ++d)
          idf[s][d] = (s == (sw >> 4) && hh == ((sw >> 3) & 1) && d == ((sw & 7) >> 1)) ? (0x3F80u << (16 * (sw & 1))) : 0u;
      lds_st(smem + LDS_ID + lane * 16, idf[0]); lds_st(smem + LDS_ID + 1024 + lane * 16, idf[1]);
    }
    asm volatile("s_waitcnt vmcnt(13) lgkmcnt(0)\n\ts_barrier" ::: "memory");
    PSTAMP(1);
    u32x4 fa[18], fb[6], res[6];
    if (wid8 < 6) {
      const int T1 = wid8 % 3;
      const lds_char* aimg = smem + LDS_RAW + (wid8 < 3 ? 384 : 0);
#pragma unroll
      for (int Tin = 0; Tin < 3; ++Tin) fold_read_rows(fa + 6 * Tin, aimg, 576, 32 * Tin, sw, hh);
      if (wid8 < 3) fold_read_cols(fb, smem + LDS_RAW + 3 * IMG, T1, lane);
      else fold_read_rows(fb, smem + LDS_RAW + 192, 576, 32 * T1, sw, hh);
      asm volatile("s_waitcnt vmcnt(8) lgkmcnt(0)\n\ts_barrier" ::: "memory");
      PSTAMP(2);
      fold_compute(fa, fb, wid8 < 3 ? 1.f : QK_SCALE, res);
      cnt_wait(l16, C_QK, 2);
      fold_store(smem + LDS_W0 + (wid8 < 3 ? IMG : 0) + T1 * 6144 + lane * 16, res);
      cnt_arrive(l16, dvoff, C_TW);
    } else {
      asm volatile("s_waitcnt vmcnt(8) lgkmcnt(0)\n\ts_barrier" ::: "memory");
      PSTAMP(2);
      u32x4 fb2[6];
      const int T1 = wid8 == 6 ? 0 : 2;
#pragma unroll
      for (int Tin = 0; Tin < 3; ++Tin) fold_read_rows(fa + 6 * Tin, smem + LDS_W0, 384, 32 * Tin, sw, hh);
      fold_read_rows(fb, smem + LDS_W0 + 192, 384, 32 * T1, sw, hh);
      fold_read_rows(fb2, smem + LDS_W0 + 192, 384, 32, sw, hh);
      cnt_arrive(l16, dvoff, C_QK);
#pragma unroll 3
      for (int i = 0; i < 18; ++i) {
        if (wid8 == 6) raw_piece<12, 576>(wqkv_s + 384, i, lane, smem_a + LDS_O);
        else raw_piece<12, 192>(wo_s, i, lane, smem_a + LDS_O + IMG);
      }
      fold_compute(fa, fb, QK_SCALE, res);
      fold_store(smem + LDS_W2 + T1 * 6144 + lane * 16, res);
      if (wid8 == 6) { fold_compute(fa, fb2, QK_SCALE, res); fold_store(smem + LDS_W2 + 6144 + lane * 16, res); }
      cnt_arrive(l16, dvoff, C_MS);
      VMWAIT(0);
      cnt_arrive(l16, dvoff, C_RAW);
    }
    PSTAMP(3);
  }

  const int cW = 1 + 2 * grp, cR = 2 + 2 * grp;
  auto laddr = [&](unsigned off) __attribute__((always_inline)) NOPK { off += smem_a; asm("" : "+v"(off)); return (lds_char*)(size_t)off; };
  const unsigned xg = LDS_X + grp * 4 * XREG;
  lds_char* xw = laddr(xg + f * XREG + (hh * 32 + 8 * wid + p) * 16);
  const lds_char* xr = laddr(xg + wid * XREG + lane * 16);
  const lds_char* xl = laddr(xg + wid * XREG + f * XFS + p * ROWB + hh * 16);
  lds_char* ow0 = laddr(LDS_O + wid8 * OSTG + r * 64 + 16 * (hh ^ ((r >> 1) & 3)));
  lds_char* ow1 = laddr(LDS_O + wid8 * OSTG + r * 64 + 16 * ((2 + hh) ^ ((r >> 1) & 3)));
  const lds_char* ord = laddr(LDS_O + wid8 * OSTG + lane * 16);
  const f32x16 zero = {};
  Ctx c;
  c.wa = l16; c.wb = laddr(WB_BASE + lane * 16);
  {
    c.ms0 = (p >> 1) & 1;
    c.ms1 = (p >> 2) & 1;
    const bool valid = (hh == (p & 1));
#pragma unroll
    for (int g = 0; g < 4; ++g) c.mg[g] = valid && (p >> 1) == g;
  }
  unsigned ooff = (lane >> 5) * 2 * IMGROWB + ((lane >> 2) & 7) * ROWB + 16 * ((lane & 3) ^ ((lane >> 3) & 3));
  asm("" : "+v"(ooff));
  auto orsrc = __builtin_amdgcn_make_buffer_rsrc((void*)out, 0, 4 * FRAMEB, 0x00020000);

  f32x16 xsA[3], xsB[3], xsC[3]; Stats stA, stB, stC; u32x4 hfA[6];
  u32x4 x1[2][6], h2[2][6];
  u32x4 ring[RD];
  bool first = true, cold = true;
  unsigned ph = 0, pk;
  const char* nxu; int nstep; bool more; unsigned hepoch;

  auto cold_start2 = [&]() __attribute__((always_inline)) NOPK {
    const int wy = unit / NWIN, wx = unit % NWIN;
    u32x4 xin[6];
    PSTAMP(7);
#pragma unroll
    for (int s = 0; s < 6; ++s) xin[s] = lds_ld(xl + 32 * s);
    LGWAIT();
    dma_tile(x + (size_t)(wy * 8 + 2 * wid + 1) * IMGROWB + (size_t)(wx * 8) * ROWB, dvoff, xr_a);
    stat_pkts<0, 24>(xin, xsA, stA);
    stat_fin(stA);
#pragma unroll
    for (int k = 0; k < 4; ++k) nrm_pkt(xsA, hfA, stA, k);
    PSTAMP(8);
    cnt_wait(l16, C_TW, 6);
#pragma unroll
    for (int n = 0; n < RD; ++n) ring[n] = wload(c, n);
  };
  auto next_setup = [&]() __attribute__((always_inline)) NOPK {
    more = nmode != NONE;
    if (nmode == SONLY) { nxu = ws + SCR_OFF + (size_t)(((nunit - 3 * NWG) * 4 + wid) * 2) * 1536; nstep = 1536; }
    else { const int ny = nunit / NWIN, nx = nunit % NWIN; nxu = x + (size_t)(ny * 8 + 2 * wid) * IMGROWB + (size_t)(nx * 8) * ROWB; nstep = IMGROWB; }
  };
  auto phaseT = [&]() __attribute__((always_inline)) NOPK {
    STAMP(1);
    TState t2;
    {
      TState t; u32x4 xin[6];
      t.hf[0] = hfA[0];
      temporal_slots<0, 0, 11, 10, false, false>(c, ring, t, xsA, stA, xin, xsB, stB, t2.hf, xsA, xw);
      if (cold) { VMWAIT(0); } else { VMWAIT(12); }
#pragma unroll
      for (int s = 0; s < 6; ++s) xin[s] = lds_ld(xl + 32 * s);
      cnt_arrive(l16, dvoff, cR);
      temporal_slots<0, 11, 46, 10, false, false>(c, ring, t, xsA, stA, xin, xsB, stB, t2.hf, xsA, xw);
      pk = cnt_peek(l16);
      temporal_slots<0, 46, 48, 10, false, false>(c, ring, t, xsA, stA, xin, xsB, stB, t2.hf, xsA, xw);
    }
    STAMP(2);
    if (first && grp == 1 && wid != 2) {
      const int T2 = wid == 3 ? 2 : wid;
      cnt_wait(l16, C_RAW, 2);
      u32x4 fb[6];
      unsigned d16 = dvoff; asm volatile("" : "+v"(d16));
      const int ln = d16 >> 4, r2 = ln & 31, sw2 = (r2 & 0x13) | ((r2 & 4) << 1) | ((r2 & 8) >> 1);
      fold_read_cols(fb, smem + LDS_O + IMG, T2, ln);
#pragma unroll
      for (int Tin = 0; Tin < 3; ++Tin) {
        u32x4 fa[6];
        fold_read_rows(fa, smem + LDS_O, 192, 32 * Tin, sw2, ln >> 5);
        f32x16 acc = zero;
#pragma unroll
        for (int s = 0; s < 6; ++s) acc = MFMA(fa[s], fb[s], acc);
        lds_char* wdst = smem + LDS_W2 + IMG + T2 * 6144 + Tin * 2048 + d16;
        lds_st(wdst, pack8(acc, 0)); lds_st(wdst + 1024, pack8(acc, 1));
      }
      cnt_arrive(l16, dvoff, C_VS);
    }
    {
      temporal_slots<36, 0, 2, 19, true, true>(c, ring, t2, xsB, stB, x1[0], xsC, stC, h2[0], xsA, xw);
      cnt_wait(l16, cR, ph + 4, pk);
      temporal_slots<36, 2, 8, 19, true, true>(c, ring, t2, xsB, stB, x1[0], xsC, stC, h2[0], xsA, xw);
      cnt_arrive(l16, dvoff, cW);
      temporal_slots<36, 8, 17, 19, true, true>(c, ring, t2, xsB, stB, x1[0], xsC, stC, h2[0], xsA, xw);
      pk = cnt_peek(l16);
      temporal_slots<36, 17, 20, 19, true, true>(c, ring, t2, xsB, stB, x1[0], xsC, stC, h2[0], xsA, xw);
      STAMP(3);
      cnt_wait(l16, cW, ph + 4, pk);
#pragma unroll
      for (int s = 0; s < 6; ++s) x1[0][s] = lds_ld(xr + s * 1024);
      cnt_arrive(l16, dvoff, cR);
      if (first) cnt_wait(l16, C_MS, 2);
      STAMP(4);
      temporal_slots<36, 20, 43, 19, true, true>(c, ring, t2, xsB, stB, x1[0], xsC, stC, h2[0], xsA, xw);
      pk = cnt_peek(l16);
      temporal_slots<36, 43, 46, 19, true, true>(c, ring, t2, xsB, stB, x1[0], xsC, stC, h2[0], xsA, xw);
      cnt_wait(l16, cR, ph + 8, pk);
      temporal_slots<36, 46, 48, 19, true, true>(c, ring, t2, xsB, stB, x1[0], xsC, stC, h2[0], xsA, xw);
    }
    STAMP(5);
  };
  auto phaseS = [&](auto SOC) __attribute__((always_inline)) NOPK {
    constexpr bool SO = SOC.v != 0;
    const int wy = unit / NWIN, wx = unit % NWIN;
    f32x16 sa[2][2]; u32x4 gq[6];
    {
      f32x16 g[3], xsD[3]; Stats stD;
      sfor<0, 10>(LAM(M) {
        constexpr int m = M.v, n = 72 + m, T = m / 6, s = m % 6;
        g[T] = MFMA(WUSE(n), h2[0][s], s == 0 ? zero : g[T]); WNEXT(n);
        SB();
        if constexpr (m < 2) {
          if constexpr (!SO) {
            u32x4 a, b; cvt4(a, xsB[1 + m], 0); lds_st(xw + (2 + 2 * m) * 1024, a); cvt4(b, xsB[1 + m], 1); lds_st(xw + (3 + 2 * m) * 1024, b);
            if constexpr (m == 1) cnt_arrive(l16, dvoff, cW);
          }
        }
        if constexpr (m < 5) {
#pragma unroll
          for (int k = 4 + 4 * m; k < 8 + 4 * m; ++k) nrm_pkt(xsC, h2[0], stC, k);
        }
        if constexpr (m == 7) { cvt4(gq[0], g[0], 0); cvt4(gq[1], g[0], 1); }
        if constexpr (!SO && m == 8) pk = cnt_peek(l16);
        SB();
      });
      STAMP(6);
      if constexpr (!SO) {
        cnt_wait(l16, cW, ph + 8, pk);
#pragma unroll
        for (int s = 0; s < 6; ++s) x1[1][s] = lds_ld(xr + s * 1024);
      } else {
        VMWAIT(12);
#pragma unroll
        for (int s = 0; s < 6; ++s) x1[1][s] = lds_ld(xl + 32 * s);
      }
      STAMP(7);
      sfor<10, 24>(LAM(M) {
        constexpr int m = M.v;
        if constexpr (m < 12) { constexpr int n = 72 + m; g[1] = MFMA(WUSE(n), h2[0][m - 6], g[1]); WNEXT(n); }
        else if constexpr (m < 14) sa[0][0] = MFMA(h2[0][m - 12], gq[m - 12], m == 12 ? zero : sa[0][0]);
        else if constexpr (m < 20) { constexpr int n = 72 + m - 2; g[2] = MFMA(WUSE(n), h2[0][m - 14], m == 14 ? zero : g[2]); WNEXT(n); }
        else sa[0][0] = MFMA(h2[0][m - 18], gq[m - 18], sa[0][0]);
        SB();
        if constexpr (m == 13) {
          LGWAIT();
          if (!SO && nmode == SONLY) {
            for (int spin = 0; spin < (1 << 22); ++spin) {
              if (__builtin_amdgcn_readfirstlane(__hip_atomic_load(hfl + 32, __ATOMIC_RELAXED, __HIP_MEMORY_SCOPE_AGENT) - hepoch) == 1u) break;
              __builtin_amdgcn_s_sleep(8);
            }
            asm volatile("" ::: "memory");
            if (lane == 0) __hip_atomic_store(hfl, hepoch + 1u, __ATOMIC_RELAXED, __HIP_MEMORY_SCOPE_AGENT);
            dma_tile_sc1(nxu, dvoff, xr_a);
          } else if (!SO && more) dma_tile(nxu, dvoff, xr_a);
        }
        if constexpr (m == 14) { cvt4(gq[2], g[1], 0); cvt4(gq[3], g[1], 1); }
        if constexpr (m == 21) { cvt4(gq[4], g[2], 0); cvt4(gq[5], g[2], 1); }
        stat_pkts<sched_cnt(m, 11, 20, 24), sched_cnt(m + 1, 11, 20, 24)>(x1[1], xsD, stD);
        if constexpr (m == 19) stat_fin(stD);
        if constexpr (m >= 20) {
#pragma unroll
          for (int k = 6 * (m - 20); k < 6 * (m - 19); ++k) nrm_pkt(xsD, h2[1], stD, k);
        }
        SB();
      });
    }
    STAMP(8);
    u32x4 pf[2][4];
    if (!SO && ph < 16) cnt_wait(l16, C_VS, 3);
    {
      f32x16 g[3]; u32x4 gp[2][2]; SMState sm;
      sfor<0, 36>(LAM(M) {
        constexpr int m = M.v;
        if constexpr (m < 6) sa[0][1] = MFMA(h2[1][m], gq[m], m == 0 ? zero : sa[0][1]);
        else if constexpr (m < 18) { constexpr int n = 90 + m - 6, T = (m - 6) / 6, s = (m - 6) % 6; g[T] = MFMA(WUSE(n), h2[1][s], s == 0 ? zero : g[T]); WNEXT(n); }
        else if constexpr (m < 22) { constexpr int kt = (m - 18) & 1, s = (m - 18) >> 1; sa[1][kt] = MFMA(h2[kt][s], gp[0][s], s == 0 ? zero : sa[1][kt]); }
        else if constexpr (m < 28) { constexpr int n = 90 + m - 10, s = m - 22; g[2] = MFMA(WUSE(n), h2[1][s], s == 0 ? zero : g[2]); WNEXT(n); }
        else { constexpr int T = (m - 28) / 4 + 1, kt = (m - 28) & 1, s = ((m - 28) >> 1) & 1; sa[1][kt] = MFMA(h2[kt][2 * T + s], gp[T & 1][s], sa[1][kt]); }
        SB();
        if constexpr (m == 14) { cvt4(gp[0][0], g[0], 0); cvt4(gp[0][1], g[0], 1); }
        if constexpr (m == 20) { cvt4(gp[1][0], g[1], 0); cvt4(gp[1][1], g[1], 1); }
        if constexpr (m == 30) { cvt4(gp[0][0], g[2], 0); cvt4(gp[0][1], g[2], 1); }
        if constexpr (m == 2 || m == 3) sm_unit<m - 2>(sa[0][0], sa[0][1], sm, pf[0]);
        if constexpr (m >= 8) sm_unit<m - 6>(sa[0][0], sa[0][1], sm, pf[0]);
        SB();
      });
    }
    STAMP(9);
    u32x4 vp[3][4], idf[2]; f32x16 vlast;
    {
      f32x16 v[6]; SMState sm;
      sfor<0, 36>(LAM(M) {
        constexpr int m = M.v, ch = m / 6, s = m % 6, kt = ch & 1, n = 108 + m;
        v[ch] = MFMA(h2[kt][s], WUSE(n), s == 0 ? zero : v[ch]); WNEXT(n);
        SB();
        if constexpr (m >= 2 && m < 32) sm_unit<m - 2>(sa[1][0], sa[1][1], sm, pf[1]);
        if constexpr (ch > 0 && (s == 2 || s == 3)) cvt4(vp[(ch - 1) >> 1][2 * ((ch - 1) & 1) + s - 2], v[ch - 1], s - 2);
        if constexpr (m == 33) { idf[0] = lds_ld(c.wa + LDS_ID); idf[1] = lds_ld(c.wa + LDS_ID + 1024); }
        SB();
      });
      vlast = v[5];
    }
    STAMP(10);
    {
      u32x4 xin[6];
      QSTAMP(1);
      if (!SO && more) {
        VMWAIT(0);
        QSTAMP(2);
#pragma unroll
        for (int s = 0; s < 6; ++s) xin[s] = lds_ld(xl + 32 * s);
        LGWAIT();
        if (nmode == SONLY) dma_tile_sc1(nxu + nstep, dvoff, xr_a); else dma_tile(nxu + nstep, dvoff, xr_a);
      } else {
#pragma unroll
        for (int s = 0; s < 6; ++s) xin[s] = u32x4{};
      }
      const unsigned obase = wid * FRAMEB + (wy * 8) * IMGROWB + (wx * 8) * ROWB;
      f32x16 o[6]; u32x4 ov[2];
      QSTAMP(3);
      sfor<0, 44>(LAM(M) {
        constexpr int m = M.v, j = m / 6, k = m % 6;
        if constexpr (m < 36) {
          constexpr int q = j / 3, T = j % 3;
          if constexpr (k < 2) o[j] = MFMA(idf[k], x1[q][2 * T + k], k == 0 ? zero : o[j]);
          else o[j] = MFMA(vp[T][k - 2], pf[q][k - 2], o[j]);
          SB();
        }
        if constexpr (m == 2 || m == 3) cvt4(vp[2][m], vlast, m - 2);
        if constexpr (j >= 2 && k == 1) {
          constexpr int jj = j - 2, q = jj / 3, T = jj % 3;
          SB();
          __builtin_amdgcn_raw_buffer_store_b128(ov[0], orsrc, ooff, obase + q * IMGROWB + 64 * T, STORE_AUX);
          __builtin_amdgcn_raw_buffer_store_b128(ov[1], orsrc, ooff, obase + (4 + q) * IMGROWB + 64 * T, STORE_AUX);
          SB(); asm volatile("s_nop 1" ::: "memory"); SB();
        }
        if constexpr (j >= 1 && j <= 6) {
          constexpr int jj = j - 1;
          constexpr int ob = (jj & 1) * 2048;
          if constexpr (k == 2) { u32x4 a; cvt4(a, o[jj], 0); lds_st(ow0 + ob, a); }
          if constexpr (k == 3) { u32x4 a; cvt4(a, o[jj], 1); lds_st(ow1 + ob, a); asm volatile("" ::: "memory"); }
          if constexpr (k == 4) { ov[0] = lds_ld(ord + ob); ov[1] = lds_ld(ord + ob + 1024); asm volatile("" ::: "memory"); }
        }
        if constexpr (!SO) stat_pkts<sched_cnt(m, 2, 34, 24), sched_cnt(m + 1, 2, 34, 24)>(xin, xsA, stA);
        if constexpr (!SO && m == 34) stat_fin(stA);
        if constexpr (!SO && (m == 36 || m == 37)) { nrm_pkt(xsA, hfA, stA, 2 * (m - 36)); nrm_pkt(xsA, hfA, stA, 2 * (m - 36) + 1); }
        if constexpr (k == 5 || m == 43) QSTAMP(4 + j);
        SB();
      });
    }
    STAMP(11);
  };

  VMWAIT(0);
  asm volatile("" : "+v"(hev));
  hepoch = __builtin_amdgcn_readfirstlane(hev);
  if (mode == TONLY) {
    next_setup();
    cold_start2();
    phaseT();
#pragma unroll
    for (int s = 2; s < 6; ++s) lds_st(xw + s * 1024, pack8(xsB[s >> 1], s & 1));
    cnt_arrive(l16, dvoff, cW);
    cnt_wait(l16, cW, ph + 8);
#pragma unroll
    for (int s = 0; s < 6; ++s) x1[1][s] = lds_ld(xr + s * 1024);
    const int hidx = (unit - 3 * NWG) * 4 + wid;
    auto srs = __builtin_amdgcn_make_buffer_rsrc((void*)(ws + SCR_OFF), 0, 4 * FRAMEB, 0x00020000);
#pragma unroll
    for (int i = 0; i < 2; ++i) {
#pragma unroll
      for (int s = 0; s < 6; ++s) lds_st((lds_char*)xl + 32 * s, x1[i][s]);
      asm volatile("" ::: "memory");
#pragma unroll
      for (int k = 0; k < 6; ++k) {
        const unsigned q = k * 64 + lane, row = (q * 683u) >> 16, n = q - 96u * row;
        u32x4 v = lds_ld(smem + LDS_X + wid8 * XREG + row * XFS + 16 * n);
        __builtin_amdgcn_raw_buffer_store_b128(v, srs, row * FRAMEB + 16 * n + (hidx * 2 + i) * 1536, 0, 16);
      }
      LGWAIT();
    }
    PSTAMP(10);
    dma_tile(nxu, dvoff, xr_a);
    ph += 8; unit = nunit; nunit = bid + 2 * NWG; first = false;
    VMWAIT(0);
    PSTAMP(11);
    if (lane == 0) __hip_atomic_store(hfl + 32, hepoch + 1u, __ATOMIC_RELAXED, __HIP_MEMORY_SCOPE_AGENT);
  }
  cold_start2();
  for (;;) {
    next_setup();
    phaseT();
    phaseS(IC<0>{});
    if (nmode != FULL) break;
    ph += 8;
    unit = nunit; nmode = lastmode; nunit = 3 * NWG + bid - 16;
    first = false; cold = false;
  }
  if (nmode == SONLY) {
    unit = nunit;
#pragma unroll
    for (int T = 0; T < 3; ++T) xsC[T] = xsA[T];
    stC = stA; h2[0][0] = hfA[0];
#pragma unroll
    for (int s = 0; s < 6; ++s) x1[0][s] = pack8(xsA[s >> 1], s & 1);
#pragma unroll
    for (int n = 0; n < RD; ++n) ring[n] = wload(c, 72 + n);
    phaseS(IC<1>{});
  }
}

extern "C" void kernel_launch(void* const* d_in, const int* in_sizes, int n_in, void* d_out, int out_size,
                              void* d_ws, size_t ws_size, hipStream_t stream) {
  (void)hipFuncSetAttribute((const void*)swin_kernel, hipFuncAttributeMaxDynamicSharedMemorySize, LDS_TOTAL);
  swin_kernel<<<NWG, 512, LDS_TOTAL, stream>>>((const char*)d_in[0], (const char*)d_in[1], (const char*)d_in[2],
                                               (const char*)d_in[3], (const char*)d_in[4], (char*)d_out, (char*)d_ws);
}
```

```cpp
#include <hip/hip_runtime.h>
#include <hip/hip_bf16.h>
#include <stdint.h>

using bf16x8 = __attribute__((ext_vector_type(8))) short;
using f32x16 = __attribute__((ext_vector_type(16))) float;
using u32x4 = __attribute__((ext_vector_type(4))) unsigned;
using f32x2 = __attribute__((ext_vector_type(2))) float;
using bf2 = __attribute__((ext_vector_type(2))) __bf16;
#if defined(__HIP_DEVICE_COMPILE__)
#define NOPK __attribute__((target("no-packed-fp32-ops")))
#else
#define NOPK
#endif
#define DI __device__ __forceinline__ NOPK
#define MFMA(a, b, c) __builtin_amdgcn_mfma_f32_32x32x16_bf16(__builtin_bit_cast(bf16x8, (a)), __builtin_bit_cast(bf16x8, (b)), (c), 0, 0, 0)
#define SB() __builtin_amdgcn_sched_barrier(0)
#define PIN(x) asm volatile("" : "+v"(x))
#define STORE_AUX 16

constexpr int CH = 96, HH = 224, WW = 224;
constexpr int ROWB = CH * 2;
constexpr int FRAMEB = HH * WW * ROWB;
constexpr int IMGROWB = WW * ROWB;
constexpr int NWIN = 28;
constexpr int NUNITS = NWIN * NWIN;
constexpr int WMAT_BYTES = 18 * 1024;
constexpr float LN_EPS = 1e-5f;
constexpr int NWG = 256;
constexpr float QK_SCALE = 0.10206207261596575f * 1.4426950408889634f;

template <int I> struct IC { static constexpr int v = I; };
template <int B, int E, class F> DI void sfor(F&& f) { if constexpr (B < E) { f(IC<B>{}); sfor<B + 1, E>(f); } }
#define LAM(M) [&](auto M) __attribute__((always_inline)) NOPK

DI unsigned pk2(float a, float b) {
  f32x2 v = {a, b};
  return __builtin_bit_cast(unsigned, __builtin_convertvector(v, bf2));
}
DI float lo16(unsigned u) { return __builtin_bit_cast(float, u << 16); }
DI float hi16(unsigned u) { return __builtin_bit_cast(float, u & 0xffff0000u); }
DI unsigned fbits(float f) { return __builtin_bit_cast(unsigned, f); }
DI float bitsf(unsigned u) { return __builtin_bit_cast(float, u); }
DI float xhalf_sum(float v) {
  auto r = __builtin_amdgcn_permlane32_swap(fbits(v), fbits(v), false, false);
  return bitsf(r[0]) + bitsf(r[1]);
}
DI float xhalf_max(float v) {
  auto r = __builtin_amdgcn_permlane32_swap(fbits(v), fbits(v), false, false);
  return __builtin_fmaxf(bitsf(r[0]), bitsf(r[1]));
}
DI float bsel(unsigned m, float a, float b) { return bitsf((m & fbits(a)) | (~m & fbits(b))); }
DI float max3(float a, float b, float c) { return __builtin_fmaxf(__builtin_fmaxf(a, b), c); }
DI u32x4 pack8(const f32x16& a, int s) {
  u32x4 r;
  r[0] = pk2(a[8 * s + 0], a[8 * s + 1]);
  r[1] = pk2(a[8 * s + 2], a[8 * s + 3]);
  r[2] = pk2(a[8 * s + 4], a[8 * s + 5]);
  r[3] = pk2(a[8 * s + 6], a[8 * s + 7]);
  return r;
}

typedef __attribute__((address_space(3))) char lds_char;
typedef __attribute__((address_space(3))) unsigned lds_u32;
DI u32x4 lds_ld(const lds_char* p) { return *(const __attribute__((address_space(3))) u32x4*)p; }
DI void lds_st(lds_char* p, u32x4 v) { *(__attribute__((address_space(3))) u32x4*)p = v; }

constexpr int LDS_CNT = 0;
DI void cnt_arrive(const lds_char* l16, unsigned dvoff, int k) {
  asm volatile("s_lshl_b64 exec, 1, %2\n\tds_add_u32 %0, %1 offset:%3\n\ts_mov_b64 exec, -1"
               :: "v"((unsigned)(size_t)l16), "v"(dvoff), "s"(k), "i"(LDS_CNT) : "memory", "scc");
}
DI unsigned cnt_peek(const lds_char* l16) {
  return __hip_atomic_load((const lds_u32*)(l16 + LDS_CNT), __ATOMIC_RELAXED, __HIP_MEMORY_SCOPE_WORKGROUP);
}
DI void cnt_wait(const lds_char* l16, int k, unsigned n, unsigned peeked) {
  const unsigned target = n * 16u * (unsigned)k;
  asm volatile("" ::: "memory");
  if (__builtin_expect((unsigned)__builtin_amdgcn_readlane(peeked, k) < target, 0)) {
    for (;;) {
      __builtin_amdgcn_s_sleep(1);
      if ((unsigned)__builtin_amdgcn_readlane(cnt_peek(l16), k) >= target) break;
    }
  }
  asm volatile("" ::: "memory");
}
DI void cnt_wait(const lds_char* l16, int k, unsigned n) { cnt_wait(l16, k, n, cnt_peek(l16)); }

constexpr int XFS = 1552;
constexpr int XREG = 6304;
constexpr int OSTG = 4096;
constexpr int LDS_ID = 256;
constexpr int LDS_W0 = LDS_ID + 2048;
constexpr int LDS_X = LDS_W0 + 2 * WMAT_BYTES;
constexpr int LDS_W2 = LDS_X + 8 * XREG;
constexpr int LDS_O = LDS_W2 + 2 * WMAT_BYTES;
constexpr int LDS_RAW = LDS_W2;
constexpr int LDS_RAW_END = LDS_RAW + 73728;
constexpr int LDS_TOTAL = LDS_RAW_END;
static_assert(LDS_O + 8 * OSTG <= LDS_RAW_END && LDS_O + 2 * WMAT_BYTES <= LDS_RAW_END, "staging");
static_assert(LDS_TOTAL <= 163840, "lds");

DI void dma_tile(const char* row, unsigned voff, unsigned lds) {
  const char* r1 = row + FRAMEB; const char* r2 = row + 2 * (size_t)FRAMEB; const char* r3 = row + 3 * (size_t)FRAMEB;
  unsigned keep;
  asm volatile(
      "s_mov_b32 %0, m0\n\t"
      "s_mov_b32 m0, %6\n\ts_nop 0\n\t"
      "global_load_lds_dwordx4 %1, %2 nt\n\t"
      "s_add_u32 m0, m0, %7\n\ts_nop 0\n\t"
      "global_load_lds_dwordx4 %1, %3 nt\n\t"
      "s_add_u32 m0, m0, %7\n\ts_nop 0\n\t"
      "global_load_lds_dwordx4 %1, %4 nt\n\t"
      "s_add_u32 m0, m0, %7\n\ts_nop 0\n\t"
      "global_load_lds_dwordx4 %1, %5 nt\n\t"
      "s_mov_b32 exec_hi, 0\n\t"
      "global_load_lds_dwordx4 %1, %5 offset:1024 nt\n\t"
      "s_sub_u32 m0, m0, %7\n\ts_nop 0\n\t"
      "global_load_lds_dwordx4 %1, %4 offset:1024 nt\n\t"
      "s_sub_u32 m0, m0, %7\n\ts_nop 0\n\t"
      "global_load_lds_dwordx4 %1, %3 offset:1024 nt\n\t"
      "s_sub_u32 m0, m0, %7\n\ts_nop 0\n\t"
      "global_load_lds_dwordx4 %1, %2 offset:1024 nt\n\t"
      "s_mov_b32 exec_hi, -1\n\t"
      "s_mov_b32 m0, %0"
      : "=&s"(keep) : "v"(voff), "s"(row), "s"(r1), "s"(r2), "s"(r3), "s"(lds), "i"(XFS) : "memory", "scc");
}
DI void dma_piece(const char* base, unsigned voff, unsigned lds) {
  unsigned keep;
  asm volatile("s_mov_b32 %0, m0\n\ts_mov_b32 m0, %3\n\ts_nop 0\n\tglobal_load_lds_dwordx4 %1, %2\n\ts_mov_b32 m0, %0"
               : "=&s"(keep) : "v"(voff), "s"(base), "s"(lds) : "memory");
}
#define VMWAIT(n) asm volatile("s_waitcnt vmcnt(" #n ")" ::: "memory")
#define LGWAIT() asm volatile("s_waitcnt lgkmcnt(0)" ::: "memory")

#define STAMP(i) do {} while (0)
#define PSTAMP(i) do {} while (0)
#define QSTAMP(i) do {} while (0)

template <int CPR, int SS>
DI void raw_piece(const char* src, int piece, int lane, unsigned lds_img) {
  const unsigned q = (unsigned)(piece * 64 + lane);
  const unsigned row = (q * (unsigned)(((1 << 20) + CPR - 1) / CPR)) >> 20;
  const unsigned off = (SS == CPR * 16) ? 16u * (q ^ ((row >> 2) & 3u)) : row * (unsigned)(SS - CPR * 16) + 16u * (q ^ ((row >> 2) & 3u));
  dma_piece(src, off, lds_img + piece * 1024);
}
typedef short s16x4 __attribute__((ext_vector_type(4)));
DI void fold_read_rows(u32x4* d, const lds_char* img, int rb, int row0, int sw, int hh) {
  const int x = (sw >> 2) & 3;
  const lds_char* b0 = img + (row0 + sw) * rb + 16 * (hh ^ x);
  const lds_char* b1 = img + (row0 + sw) * rb + 16 * ((2 + hh) ^ x);
#pragma unroll
  for (int s = 0; s < 6; ++s) d[s] = lds_ld(((s & 1) ? b1 : b0) + 64 * (s >> 1));
}
DI void fold_read_cols(u32x4* d, const lds_char* img, int T, int lane) {
  const int hh = lane >> 5, cb = (lane >> 4) & 1, qq = (lane >> 2) & 3, pp = lane & 3;
  const int cc = 4 * T + 2 * cb + (pp & 1);
  const lds_char* b0 = img + (8 * hh + qq) * 192 + 16 * (cc ^ ((2 * hh) & 3)) + 8 * (pp >> 1);
  const lds_char* b1 = img + (8 * hh + qq + 4) * 192 + 16 * (cc ^ ((2 * hh + 1) & 3)) + 8 * (pp >> 1);
#pragma unroll
  for (int s = 0; s < 6; ++s) {
    s16x4 lo = __builtin_amdgcn_ds_read_tr16_b64_v4i16((__attribute__((address_space(3))) s16x4*)(b0 + s * 16 * 192));
    s16x4 hi = __builtin_amdgcn_ds_read_tr16_b64_v4i16((__attribute__((address_space(3))) s16x4*)(b1 + s * 16 * 192));
    d[s] = __builtin_bit_cast(u32x4, __builtin_shufflevector(lo, hi, 0, 1, 2, 3, 4, 5, 6, 7));
  }
}
DI void fold_compute(const u32x4 (&a)[18], const u32x4 (&b)[6], float sc, u32x4 (&res)[6]) {
  const f32x16 zero = {};
  f32x16 acc[3];
#pragma unroll
  for (int s = 0; s < 6; ++s)
#pragma unroll
    for (int Tin = 0; Tin < 3; ++Tin) acc[Tin] = MFMA(a[Tin * 6 + s], b[s], s == 0 ? zero : acc[Tin]);
#pragma unroll
  for (int Tin = 0; Tin < 3; ++Tin) {
#pragma unroll
    for (int i = 0; i < 16; ++i) acc[Tin][i] *= sc;
    res[2 * Tin] = pack8(acc[Tin], 0);
    res[2 * Tin + 1] = pack8(acc[Tin], 1);
  }
}
DI void fold_store(lds_char* wdst, const u32x4 (&res)[6]) {
#pragma unroll
  for (int s = 0; s < 6; ++s) lds_st(wdst + s * 1024, res[s]);
}

struct Stats { float s0, s1, q0, q1, rs, nb; };
DI void stat_pkt(const u32x4 (&xf)[6], f32x16 (&xs)[3], Stats& st, int k) {
  const int s = k >> 2, i = k & 3, T = s >> 1, e = 8 * (s & 1) + 2 * i;
  const float a = lo16(xf[s][i]), b = hi16(xf[s][i]);
  xs[T][e] = a; xs[T][e + 1] = b;
  if (k == 0) { st.s0 = a; st.s1 = b; st.q0 = a * a; st.q1 = b * b; }
  else { st.s0 += a; st.s1 += b; st.q0 = __builtin_fmaf(a, a, st.q0); st.q1 = __builtin_fmaf(b, b, st.q1); }
  PIN(st.s0); PIN(st.s1); PIN(st.q0); PIN(st.q1);
}
DI void stat_fin(Stats& st) {
  float s = xhalf_sum(st.s0 + st.s1), q = xhalf_sum(st.q0 + st.q1);
  float m = s * (1.f / CH);
  float var = __builtin_fmaxf(__builtin_fmaf(-m, m, q * (1.f / CH)), 0.f);
  st.rs = __builtin_amdgcn_rsqf(var + LN_EPS);
  st.nb = -m * st.rs;
  PIN(st.rs); PIN(st.nb);
}
DI void nrm_pkt(const f32x16 (&xs)[3], u32x4 (&hf)[6], const Stats& st, int k) {
  const int s = k >> 2, i = k & 3, T = s >> 1, e = 8 * (s & 1) + 2 * i;
  unsigned v = pk2(__builtin_fmaf(xs[T][e], st.rs, st.nb), __builtin_fmaf(xs[T][e + 1], st.rs, st.nb));
  PIN(v);
  hf[s][i] = v;
}
DI void cvt4(u32x4& d, const f32x16& a, int s) { d = pack8(a, s); PIN(d); }

constexpr int RD = 4;
constexpr int NWSEQ = 144;
DI constexpr int woff(int n) {
  n %= NWSEQ;
  if (n < 72) {
    int k = n % 36;
    if (k < 12) return LDS_W0 + (k & 1) * WMAT_BYTES + (k >> 1) * 1024;
    if (k < 24) return LDS_W0 + (6 + (k - 12)) * 1024;
    return LDS_W0 + WMAT_BYTES + (6 + (k - 24)) * 1024;
  }
  if (n < 108) return LDS_W2 + ((n - 72) % 18) * 1024;
  return LDS_W2 + WMAT_BYTES + (((n - 108) / 12) * 6 + (n - 108) % 6) * 1024;
}
constexpr int WB_BASE = LDS_W2;
struct Ctx {
  const lds_char* wa;
  const lds_char* wb;
  bool ms0, ms1, mg[4];
};
DI u32x4 wload(const Ctx& c, int n) {
  const int o = woff(n);
  return o < LDS_W2 ? lds_ld(c.wa + o) : lds_ld(c.wb + (o - WB_BASE));
}
#define WUSE(n) ring[(n) % RD]
#define WNEXT(n) ring[(n) % RD] = wload(c, (n) + RD)

struct Slot { int ty, T, s; };
DI constexpr Slot tslot(int m) {
  if (m < 12) return {m & 1, 0, m >> 1};
  if (m < 15) return {0, 1, m - 12};
  if (m < 17) return {2, 0, m - 15};
  if (m < 20) return {0, 1, m - 14};
  if (m < 23) return {0, 2, m - 20};
  if (m < 25) return {2, 1, m - 23};
  if (m < 28) return {0, 2, m - 22};
  if (m < 31) return {1, 1, m - 28};
  if (m < 33) return {2, 2, m - 31};
  if (m < 36) return {1, 1, m - 30};
  if (m < 42) return {1, 2, m - 36};
  return {3, (m - 42) >> 1, (m - 42) & 1};
}
DI constexpr int twidx(int m) {
  return m < 15 ? m : (m < 17 ? 15 : (m < 23 ? m - 2 : (m < 25 ? 21 : (m < 31 ? m - 4 : (m < 33 ? 27 : (m < 42 ? m - 6 : 36))))));
}
DI constexpr int sched_cnt(int m, int first, int last, int total) {
  return m <= first ? 0 : (m >= last ? total : (m - first) * total / (last - first));
}

struct TState { f32x16 g[3], v[3], sa; u32x4 hf[6], gp[2], vp[3][2], pf[2]; float e[4]; unsigned pk0, pk1; };
template <int K0, int K1>
DI void stat_pkts(const u32x4 (&xf)[6], f32x16 (&xs)[3], Stats& st) {
  if constexpr (K0 < K1) { stat_pkt(xf, xs, st, K0); stat_pkts<K0 + 1, K1>(xf, xs, st); }
}
template <int N0, int m, int M1, int HS, bool PE, bool OE>
DI void temporal_slots(const Ctx& c, u32x4 (&ring)[RD], TState& t, f32x16 (&xs)[3], const Stats& st,
                       const u32x4 (&nx)[6], f32x16 (&nxs)[3], Stats& nst, u32x4 (&nhf)[6], const f32x16 (&pxs)[3], lds_char* xw) {
  const f32x16 zero = {};
  if constexpr (m < M1) {
    constexpr Slot sl = tslot(m);
    constexpr int n = N0 + twidx(m);
    if constexpr (sl.ty == 0) { t.g[sl.T] = MFMA(WUSE(n), t.hf[sl.s], sl.s == 0 ? zero : t.g[sl.T]); WNEXT(n); }
    else if constexpr (sl.ty == 1) { t.v[sl.T] = MFMA(t.hf[sl.s], WUSE(n), sl.s == 0 ? zero : t.v[sl.T]); WNEXT(n); }
    else if constexpr (sl.ty == 2) t.sa = MFMA(t.hf[2 * sl.T + sl.s], t.gp[sl.s], (sl.T == 0 && sl.s == 0) ? zero : t.sa);
    else xs[sl.T] = MFMA(t.vp[sl.T][sl.s], t.pf[sl.s], xs[sl.T]);
    SB();
    if constexpr (m < 10) { nrm_pkt(xs, t.hf, st, 4 + 2 * m); nrm_pkt(xs, t.hf, st, 5 + 2 * m); }
    if constexpr (PE && m >= 2 && m < 8) { u32x4 a; cvt4(a, pxs[(m - 2) >> 1], m & 1); lds_st(xw + (m - 2) * 1024, a); }
    if constexpr (m == 12) cvt4(t.gp[0], t.g[0], 0);
    if constexpr (m == 13) cvt4(t.gp[1], t.g[0], 1);
    if constexpr (m == 14) cvt4(t.vp[0][0], t.v[0], 0);
    if constexpr (m == 15) cvt4(t.vp[0][1], t.v[0], 1);
    if constexpr (m == 21) { cvt4(t.gp[0], t.g[1], 0); }
    if constexpr (m == 22) { cvt4(t.gp[1], t.g[1], 1); }
    if constexpr (m == 29) { cvt4(t.gp[0], t.g[2], 0); }
    if constexpr (m == 30) { cvt4(t.gp[1], t.g[2], 1); }
    if constexpr (m == 35 || m == 36) {
#pragma unroll
      for (int k = 2 * (m - 35); k < 2 * (m - 35) + 2; ++k) {
        float s1 = t.sa[4 + k], s3 = t.sa[12 + k];
        asm("" : "+v"(s1)); asm("" : "+v"(s3));
        float a = c.ms0 ? s1 : t.sa[k];
        float b = c.ms0 ? s3 : t.sa[8 + k];
        t.e[k] = c.ms1 ? b : a; PIN(t.e[k]);
      }
    }
    if constexpr (m == 37) {
      float mx = max3(max3(t.e[0], t.e[1], t.e[2]), t.e[3], t.e[3]);
#pragma unroll
      for (int k = 0; k < 4; ++k) { t.e[k] = __builtin_amdgcn_exp2f(t.e[k] - mx); PIN(t.e[k]); }
    }
    if constexpr (m == 38) {
      float rinv = __builtin_amdgcn_rcpf((t.e[0] + t.e[1]) + (t.e[2] + t.e[3]));
      t.pk0 = pk2(t.e[0] * rinv, t.e[1] * rinv); t.pk1 = pk2(t.e[2] * rinv, t.e[3] * rinv);
      PIN(t.pk0); PIN(t.pk1);
    }
    if constexpr (m == 39) {
#pragma unroll
      for (int g = 0; g < 4; ++g) {
        t.pf[g >> 1][(g & 1) * 2 + 0] = c.mg[g] ? t.pk0 : 0u;
        t.pf[g >> 1][(g & 1) * 2 + 1] = c.mg[g] ? t.pk1 : 0u;
      }
      PIN(t.pf[0]); PIN(t.pf[1]);
    }
    if constexpr (m == 40) { cvt4(t.vp[1][0], t.v[1], 0); cvt4(t.vp[1][1], t.v[1], 1); }
    if constexpr (m == 44) { cvt4(t.vp[2][0], t.v[2], 0); cvt4(t.vp[2][1], t.v[2], 1); }
    if constexpr (m < 35) stat_pkts<sched_cnt(m, HS + 2, 35, 20), sched_cnt(m + 1, HS + 2, 35, 20)>(nx, nxs, nst);
    if constexpr (m == 41) stat_pkts<20, 22>(nx, nxs, nst);
    if constexpr (m == 42) stat_pkts<22, 24>(nx, nxs, nst);
    if constexpr (m == 43) stat_fin(nst);
    if constexpr (m == 45) { nrm_pkt(nxs, nhf, nst, 0); nrm_pkt(nxs, nhf, nst, 1); }
    if constexpr (m == 46) { nrm_pkt(nxs, nhf, nst, 2); nrm_pkt(nxs, nhf, nst, 3); }
    if constexpr (OE && m == 46) { u32x4 a; cvt4(a, xs[0], 0); lds_st(xw, a); }
    if constexpr (OE && m == 47) { u32x4 a; cvt4(a, xs[0], 1); lds_st(xw + 1024, a); }
    SB();
    temporal_slots<N0, m + 1, M1, HS, PE, OE>(c, ring, t, xs, st, nx, nxs, nst, nhf, pxs, xw);
  }
}

struct SMState { float m0, m1, mx, l0, l1, rinv; };
template <int u>
DI void sm_unit(f32x16& a, f32x16& b, SMState& s, u32x4 (&pf)[4]) {
  if constexpr (u < 4) {
    f32x16& x = (u < 2) ? a : b;
    const int o = 8 * (u & 1);
    if (u == 0) { s.m0 = max3(x[0], x[1], x[2]); s.m1 = max3(x[3], x[4], x[5]); s.m0 = max3(s.m0, x[6], x[7]); }
    else { s.m0 = max3(s.m0, x[o], x[o + 1]); s.m1 = max3(s.m1, x[o + 2], x[o + 3]); s.m0 = max3(s.m0, x[o + 4], x[o + 5]); s.m1 = max3(s.m1, x[o + 6], x[o + 7]); }
    PIN(s.m0); PIN(s.m1);
  } else if constexpr (u == 4) {
    s.mx = xhalf_max(__builtin_fmaxf(s.m0, s.m1)); PIN(s.mx);
  } else if constexpr (u < 21) {
    constexpr int k = u - 5;
    f32x16& x = (k < 8) ? a : b;
    constexpr int o = 2 * (k & 7);
    float e0 = __builtin_amdgcn_exp2f(x[o] - s.mx), e1 = __builtin_amdgcn_exp2f(x[o + 1] - s.mx);
    PIN(e0); PIN(e1);
    x[o] = e0; x[o + 1] = e1;
    if constexpr (k >= 2) {
      f32x16& y = (k - 1 < 8) ? a : b;
      constexpr int o1 = 2 * ((k - 1) & 7);
      if constexpr (k == 2) { s.l0 = a[0] + y[o1]; s.l1 = a[1] + y[o1 + 1]; } else { s.l0 += y[o1]; s.l1 += y[o1 + 1]; }
      PIN(s.l0); PIN(s.l1);
    }
  } else if constexpr (u == 21) {
    s.rinv = __builtin_amdgcn_rcpf(xhalf_sum((s.l0 + b[14]) + (s.l1 + b[15]))); PIN(s.rinv);
  } else {
    const int k = u - 22;
    f32x16& x = (k < 4) ? a : b;
    const int o = 4 * (k & 3);
    unsigned p0 = pk2(x[o] * s.rinv, x[o + 1] * s.rinv), p1 = pk2(x[o + 2] * s.rinv, x[o + 3] * s.rinv);
    PIN(p0); PIN(p1);
    pf[k >> 1][2 * (k & 1)] = p0; pf[k >> 1][2 * (k & 1) + 1] = p1;
  }
}

__global__ __launch_bounds__(512, 2) NOPK void swin_kernel(const char* __restrict__ x, const char* __restrict__ wqkv_t,
                                                           const char* __restrict__ wo_t, const char* __restrict__ wqkv_s,
                                                           const char* __restrict__ wo_s, char* __restrict__ out
) {
  extern __shared__ __attribute__((aligned(16))) char smem_[];
  lds_char* smem = (lds_char*)smem_;
  const unsigned smem_a = (unsigned)(size_t)smem;
  const int tid = threadIdx.x, lane = tid & 63;
  const int wid8 = __builtin_amdgcn_readfirstlane(tid >> 6);
  const int grp = wid8 >> 2, wid = wid8 & 3;
  const int r = lane & 31, hh = lane >> 5;
  const int p = r >> 2, f = r & 3;
  const int sw = (r & 0x13) | ((r & 4) << 1) | ((r & 8) >> 1);
  const unsigned dvoff = lane * 16;
  const unsigned xr_a = smem_a + LDS_X + wid8 * XREG;

  int unit = blockIdx.x + NWG * grp;
  STAMP(0);
  const lds_char* l16 = smem + lane * 16;
  constexpr int C_TW = 5, C_MS = 6, C_RAW = 7, C_VS = 8, C_QK = 9;
  constexpr int IMG = WMAT_BYTES;
  {
    const int rot = (blockIdx.x * 11) % 72;
#pragma unroll
    for (int i = 0; i < 9; ++i) {
      int pc = wid8 * 9 + i + rot; pc -= pc >= 72 ? 72 : 0;
      if (pc < 54) raw_piece<36, 576>(wqkv_t, pc, lane, smem_a + LDS_RAW);
      else raw_piece<12, 192>(wo_t, pc - 54, lane, smem_a + LDS_RAW + 3 * IMG);
    }
#pragma unroll
    for (int i = 0; i < 5; ++i) {
      int pc = (wid8 * 5 + i + rot) % 36;
      raw_piece<24, 576>(wqkv_s, pc, lane, smem_a + LDS_W0);
    }
    if (tid < 64) ((lds_u32*)(smem + LDS_CNT))[tid] = 0u;
    asm volatile("s_barrier" ::: "memory");
    {
      const int wy = unit / NWIN, wx = unit % NWIN;
      dma_tile(x + (size_t)(wy * 8 + 2 * wid) * IMGROWB + (size_t)(wx * 8) * ROWB, dvoff, xr_a);
    }
    PSTAMP(0);
    if (wid8 == 7) {
      u32x4 idf[2];
#pragma unroll
      for (int s = 0; s < 2; ++s)
#pragma unroll
        for (int d = 0; d < 4; ++d)
          idf[s][d] = (s == (sw >> 4) && hh == ((sw >> 3) & 1) && d == ((sw & 7) >> 1)) ? (0x3F80u << (16 * (sw & 1))) : 0u;
      lds_st(smem + LDS_ID + lane * 16, idf[0]); lds_st(smem + LDS_ID + 1024 + lane * 16, idf[1]);
    }
    asm volatile("s_waitcnt vmcnt(13) lgkmcnt(0)\n\ts_barrier" ::: "memory");
    PSTAMP(1);
    u32x4 fa[18], fb[6], res[6];
    if (wid8 < 6) {
      const int T1 = wid8 % 3;
      const lds_char* aimg = smem + LDS_RAW + (wid8 < 3 ? 384 : 0);
#pragma unroll
      for (int Tin = 0; Tin < 3; ++Tin) fold_read_rows(fa + 6 * Tin, aimg, 576, 32 * Tin, sw, hh);
      if (wid8 < 3) fold_read_cols(fb, smem + LDS_RAW + 3 * IMG, T1, lane);
      else fold_read_rows(fb, smem + LDS_RAW + 192, 576, 32 * T1, sw, hh);
      asm volatile("s_waitcnt vmcnt(8) lgkmcnt(0)\n\ts_barrier" ::: "memory");
      PSTAMP(2);
      fold_compute(fa, fb, wid8 < 3 ? 1.f : QK_SCALE, res);
      cnt_wait(l16, C_QK, 2);
      fold_store(smem + LDS_W0 + (wid8 < 3 ? IMG : 0) + T1 * 6144 + lane * 16, res);
      cnt_arrive(l16, dvoff, C_TW);
    } else {
      asm volatile("s_waitcnt vmcnt(8) lgkmcnt(0)\n\ts_barrier" ::: "memory");
      PSTAMP(2);
      u32x4 fb2[6];
      const int T1 = wid8 == 6 ? 0 : 2;
#pragma unroll
      for (int Tin = 0; Tin < 3; ++Tin) fold_read_rows(fa + 6 * Tin, smem + LDS_W0, 384, 32 * Tin, sw, hh);
      fold_read_rows(fb, smem + LDS_W0 + 192, 384, 32 * T1, sw, hh);
      fold_read_rows(fb2, smem + LDS_W0 + 192, 384, 32, sw, hh);
      cnt_arrive(l16, dvoff, C_QK);
#pragma unroll 3
      for (int i = 0; i < 18; ++i) {
        if (wid8 == 6) raw_piece<12, 576>(wqkv_s + 384, i, lane, smem_a + LDS_O);
        else raw_piece<12, 192>(wo_s, i, lane, smem_a + LDS_O + IMG);
      }
      fold_compute(fa, fb, QK_SCALE, res);
      fold_store(smem + LDS_W2 + T1 * 6144 + lane * 16, res);
      if (wid8 == 6) { fold_compute(fa, fb2, QK_SCALE, res); fold_store(smem + LDS_W2 + 6144 + lane * 16, res); }
      cnt_arrive(l16, dvoff, C_MS);
      VMWAIT(0);
      cnt_arrive(l16, dvoff, C_RAW);
    }
    PSTAMP(3);
    if (grp == 1 && blockIdx.x + 3 * NWG < NUNITS) __builtin_amdgcn_s_setprio(1);
  }

  const int cW = 1 + 2 * grp, cR = 2 + 2 * grp;
  lds_char* xg = smem + LDS_X + grp * 4 * XREG;
  lds_char* xw = xg + f * XREG + (hh * 32 + 8 * wid + p) * 16;
  const lds_char* xr = xg + wid * XREG + lane * 16;
  const lds_char* xl = xg + wid * XREG + f * XFS + p * ROWB + hh * 16;
  lds_char* og = smem + LDS_O + wid8 * OSTG;
  const f32x16 zero = {};
  Ctx c;
  c.wa = l16; c.wb = smem + WB_BASE + lane * 16;
  {
    c.ms0 = (p >> 1) & 1;
    c.ms1 = (p >> 2) & 1;
    const bool valid = (hh == (p & 1));
#pragma unroll
    for (int g = 0; g < 4; ++g) c.mg[g] = valid && (p >> 1) == g;
  }
  const unsigned ost_w0 = r * 64 + 16 * ((hh) ^ ((r >> 1) & 3)), ost_w1 = ost_w0 ^ 32;
  const unsigned ooff = (lane >> 5) * 2 * IMGROWB + ((lane >> 2) & 7) * ROWB + 16 * ((lane & 3) ^ ((lane >> 3) & 3));
  auto orsrc = __builtin_amdgcn_make_buffer_rsrc((void*)out, 0, 4 * FRAMEB, 0x00020000);

  f32x16 xsA[3]; Stats stA; u32x4 hfA[6];
  u32x4 ring[RD];
  {
    u32x4 xin[6];
    PSTAMP(6);
    VMWAIT(0);
    PSTAMP(7);
#pragma unroll
    for (int s = 0; s < 6; ++s) xin[s] = lds_ld(xl + 32 * s);
    LGWAIT();
    const int wy = unit / NWIN, wx = unit % NWIN;
    dma_tile(x + (size_t)(wy * 8 + 2 * wid + 1) * IMGROWB + (size_t)(wx * 8) * ROWB, dvoff, xr_a);
    stat_pkts<0, 24>(xin, xsA, stA);
    stat_fin(stA);
#pragma unroll
    for (int k = 0; k < 4; ++k) nrm_pkt(xsA, hfA, stA, k);
    PSTAMP(8);
    cnt_wait(l16, C_TW, 6);
#pragma unroll
    for (int n = 0; n < RD; ++n) ring[n] = wload(c, n);
  }
  bool first = true;
  unsigned ph = 0;
  for (;;) {
    const int wy = unit / NWIN, wx = unit % NWIN;
    STAMP(1);
    u32x4 x1[2][6], h2[2][6];
    f32x16 xsB[3], xsC[3]; Stats stB, stC;
    TState t2;
    unsigned pk;
    {
      TState t; u32x4 xin[6];
      t.hf[0] = hfA[0];
      temporal_slots<0, 0, 11, 10, false, false>(c, ring, t, xsA, stA, xin, xsB, stB, t2.hf, xsA, xw);
      if (first) { VMWAIT(0); } else { VMWAIT(12); }
#pragma unroll
      for (int s = 0; s < 6; ++s) xin[s] = lds_ld(xl + 32 * s);
      cnt_arrive(l16, dvoff, cR);
      temporal_slots<0, 11, 46, 10, false, false>(c, ring, t, xsA, stA, xin, xsB, stB, t2.hf, xsA, xw);
      pk = cnt_peek(l16);
      temporal_slots<0, 46, 48, 10, false, false>(c, ring, t, xsA, stA, xin, xsB, stB, t2.hf, xsA, xw);
    }
    STAMP(2);
    if (first && grp == 1 && wid != 2) {
      const int T2 = wid == 3 ? 2 : wid;
      cnt_wait(l16, C_RAW, 2);
      u32x4 fb[6];
      fold_read_cols(fb, smem + LDS_O + IMG, T2, lane);
#pragma unroll
      for (int Tin = 0; Tin < 3; ++Tin) {
        u32x4 fa[6];
        fold_read_rows(fa, smem + LDS_O, 192, 32 * Tin, sw, hh);
        f32x16 acc = zero;
#pragma unroll
        for (int s = 0; s < 6; ++s) acc = MFMA(fa[s], fb[s], acc);
        lds_char* wdst = smem + LDS_W2 + IMG + T2 * 6144 + Tin * 2048 + lane * 16;
        lds_st(wdst, pack8(acc, 0)); lds_st(wdst + 1024, pack8(acc, 1));
      }
      cnt_arrive(l16, dvoff, C_VS);
    }
    {
      temporal_slots<36, 0, 2, 19, true, true>(c, ring, t2, xsB, stB, x1[0], xsC, stC, h2[0], xsA, xw);
      cnt_wait(l16, cR, ph + 4, pk);
      temporal_slots<36, 2, 8, 19, true, true>(c, ring, t2, xsB, stB, x1[0], xsC, stC, h2[0], xsA, xw);
      cnt_arrive(l16, dvoff, cW);
      temporal_slots<36, 8, 17, 19, true, true>(c, ring, t2, xsB, stB, x1[0], xsC, stC, h2[0], xsA, xw);
      pk = cnt_peek(l16);
      temporal_slots<36, 17, 20, 19, true, true>(c, ring, t2, xsB, stB, x1[0], xsC, stC, h2[0], xsA, xw);
      STAMP(3);
      cnt_wait(l16, cW, ph + 4, pk);
#pragma unroll
      for (int s = 0; s < 6; ++s) x1[0][s] = lds_ld(xr + s * 1024);
      cnt_arrive(l16, dvoff, cR);
      if (first) cnt_wait(l16, C_MS, 2);
      STAMP(4);
      temporal_slots<36, 20, 43, 19, true, true>(c, ring, t2, xsB, stB, x1[0], xsC, stC, h2[0], xsA, xw);
      pk = cnt_peek(l16);
      temporal_slots<36, 43, 46, 19, true, true>(c, ring, t2, xsB, stB, x1[0], xsC, stC, h2[0], xsA, xw);
      cnt_wait(l16, cR, ph + 8, pk);
      temporal_slots<36, 46, 48, 19, true, true>(c, ring, t2, xsB, stB, x1[0], xsC, stC, h2[0], xsA, xw);
    }
    STAMP(5);
    const int nunit = unit + 2 * NWG;
    const bool more = nunit < NUNITS;
    const int ny = nunit / NWIN, nx = nunit % NWIN;
    const char* nxu = x + (size_t)(ny * 8 + 2 * wid) * IMGROWB + (size_t)(nx * 8) * ROWB;
    f32x16 sa[2][2]; u32x4 gq[6];
    {
      f32x16 g[3], xsD[3]; Stats stD;
      sfor<0, 10>(LAM(M) {
        constexpr int m = M.v, n = 72 + m, T = m / 6, s = m % 6;
        g[T] = MFMA(WUSE(n), h2[0][s], s == 0 ? zero : g[T]); WNEXT(n);
        SB();
        if constexpr (m < 2) {
          u32x4 a, b; cvt4(a, xsB[1 + m], 0); lds_st(xw + (2 + 2 * m) * 1024, a); cvt4(b, xsB[1 + m], 1); lds_st(xw + (3 + 2 * m) * 1024, b);
          if constexpr (m == 1) cnt_arrive(l16, dvoff, cW);
        }
        if constexpr (m < 5) {
#pragma unroll
          for (int k = 4 + 4 * m; k < 8 + 4 * m; ++k) nrm_pkt(xsC, h2[0], stC, k);
        }
        if constexpr (m == 7) { cvt4(gq[0], g[0], 0); cvt4(gq[1], g[0], 1); }
        if constexpr (m == 8) pk = cnt_peek(l16);
        SB();
      });
      STAMP(6);
      cnt_wait(l16, cW, ph + 8, pk);
#pragma unroll
      for (int s = 0; s < 6; ++s) x1[1][s] = lds_ld(xr + s * 1024);
      STAMP(7);
      sfor<10, 24>(LAM(M) {
        constexpr int m = M.v;
        if constexpr (m < 12) { constexpr int n = 72 + m; g[1] = MFMA(WUSE(n), h2[0][m - 6], g[1]); WNEXT(n); }
        else if constexpr (m < 14) sa[0][0] = MFMA(h2[0][m - 12], gq[m - 12], m == 12 ? zero : sa[0][0]);
        else if constexpr (m < 20) { constexpr int n = 72 + m - 2; g[2] = MFMA(WUSE(n), h2[0][m - 14], m == 14 ? zero : g[2]); WNEXT(n); }
        else sa[0][0] = MFMA(h2[0][m - 18], gq[m - 18], sa[0][0]);
        SB();
        if constexpr (m == 13) { LGWAIT(); if (more) dma_tile(nxu, dvoff, xr_a); }
        if constexpr (m == 14) { cvt4(gq[2], g[1], 0); cvt4(gq[3], g[1], 1); }
        if constexpr (m == 21) { cvt4(gq[4], g[2], 0); cvt4(gq[5], g[2], 1); }
        stat_pkts<sched_cnt(m, 11, 20, 24), sched_cnt(m + 1, 11, 20, 24)>(x1[1], xsD, stD);
        if constexpr (m == 19) stat_fin(stD);
        if constexpr (m >= 20) {
#pragma unroll
          for (int k = 6 * (m - 20); k < 6 * (m - 19); ++k) nrm_pkt(xsD, h2[1], stD, k);
        }
        SB();
      });
    }
    STAMP(8);
    u32x4 pf[2][4];
    if (first) cnt_wait(l16, C_VS, 3);
    {
      f32x16 g[3]; u32x4 gp[2][2]; SMState sm;
      sfor<0, 36>(LAM(M) {
        constexpr int m = M.v;
        if constexpr (m < 6) sa[0][1] = MFMA(h2[1][m], gq[m], m == 0 ? zero : sa[0][1]);
        else if constexpr (m < 18) { constexpr int n = 90 + m - 6, T = (m - 6) / 6, s = (m - 6) % 6; g[T] = MFMA(WUSE(n), h2[1][s], s == 0 ? zero : g[T]); WNEXT(n); }
        else if constexpr (m < 22) { constexpr int kt = (m - 18) & 1, s = (m - 18) >> 1; sa[1][kt] = MFMA(h2[kt][s], gp[0][s], s == 0 ? zero : sa[1][kt]); }
        else if constexpr (m < 28) { constexpr int n = 90 + m - 10, s = m - 22; g[2] = MFMA(WUSE(n), h2[1][s], s == 0 ? zero : g[2]); WNEXT(n); }
        else { constexpr int T = (m - 28) / 4 + 1, kt = (m - 28) & 1, s = ((m - 28) >> 1) & 1; sa[1][kt] = MFMA(h2[kt][2 * T + s], gp[T & 1][s], sa[1][kt]); }
        SB();
        if constexpr (m == 14) { cvt4(gp[0][0], g[0], 0); cvt4(gp[0][1], g[0], 1); }
        if constexpr (m == 20) { cvt4(gp[1][0], g[1], 0); cvt4(gp[1][1], g[1], 1); }
        if constexpr (m == 30) { cvt4(gp[0][0], g[2], 0); cvt4(gp[0][1], g[2], 1); }
        if constexpr (m == 2 || m == 3) sm_unit<m - 2>(sa[0][0], sa[0][1], sm, pf[0]);
        if constexpr (m >= 8) sm_unit<m - 6>(sa[0][0], sa[0][1], sm, pf[0]);
        SB();
      });
    }
    STAMP(9);
    u32x4 vp[3][4], idf[2]; f32x16 vlast;
    {
      f32x16 v[6]; SMState sm;
      sfor<0, 36>(LAM(M) {
        constexpr int m = M.v, ch = m / 6, s = m % 6, kt = ch & 1, n = 108 + m;
        v[ch] = MFMA(h2[kt][s], WUSE(n), s == 0 ? zero : v[ch]); WNEXT(n);
        SB();
        if constexpr (m >= 2 && m < 32) sm_unit<m - 2>(sa[1][0], sa[1][1], sm, pf[1]);
        if constexpr (ch > 0 && (s == 2 || s == 3)) cvt4(vp[(ch - 1) >> 1][2 * ((ch - 1) & 1) + s - 2], v[ch - 1], s - 2);
        if constexpr (m == 33) { idf[0] = lds_ld(c.wa + LDS_ID); idf[1] = lds_ld(c.wa + LDS_ID + 1024); }
        SB();
      });
      vlast = v[5];
    }
    STAMP(10);
    {
      u32x4 xin[6];
      QSTAMP(1);
      if (more) {
        VMWAIT(0);
        QSTAMP(2);
#pragma unroll
        for (int s = 0; s < 6; ++s) xin[s] = lds_ld(xl + 32 * s);
        LGWAIT();
        dma_tile(nxu + IMGROWB, dvoff, xr_a);
      } else {
#pragma unroll
        for (int s = 0; s < 6; ++s) xin[s] = u32x4{};
      }
      const unsigned obase = wid * FRAMEB + (wy * 8) * IMGROWB + (wx * 8) * ROWB;
      f32x16 o[6]; u32x4 ov[2];
      QSTAMP(3);
      sfor<0, 44>(LAM(M) {
        constexpr int m = M.v, j = m / 6, k = m % 6;
        if constexpr (m < 36) {
          constexpr int q = j / 3, T = j % 3;
          if constexpr (k < 2) o[j] = MFMA(idf[k], x1[q][2 * T + k], k == 0 ? zero : o[j]);
          else o[j] = MFMA(vp[T][k - 2], pf[q][k - 2], o[j]);
          SB();
        }
        if constexpr (m == 2 || m == 3) cvt4(vp[2][m], vlast, m - 2);
        if constexpr (j >= 2 && k == 1) {
          constexpr int jj = j - 2, q = jj / 3, T = jj % 3;
          SB();
          __builtin_amdgcn_raw_buffer_store_b128(ov[0], orsrc, ooff, obase + q * IMGROWB + 64 * T, STORE_AUX);
          __builtin_amdgcn_raw_buffer_store_b128(ov[1], orsrc, ooff, obase + (4 + q) * IMGROWB + 64 * T, STORE_AUX);
          SB(); asm volatile("s_nop 1" ::: "memory"); SB();
        }
        if constexpr (j >= 1 && j <= 6) {
          constexpr int jj = j - 1;
          lds_char* ob = og + (jj & 1) * 2048;
          if constexpr (k == 2) { u32x4 a; cvt4(a, o[jj], 0); lds_st(ob + ost_w0, a); }
          if constexpr (k == 3) { u32x4 a; cvt4(a, o[jj], 1); lds_st(ob + ost_w1, a); asm volatile("" ::: "memory"); }
          if constexpr (k == 4) { ov[0] = lds_ld(ob + lane * 16); ov[1] = lds_ld(ob + 1024 + lane * 16); asm volatile("" ::: "memory"); }
        }
        stat_pkts<sched_cnt(m, 2, 34, 24), sched_cnt(m + 1, 2, 34, 24)>(xin, xsA, stA);
        if constexpr (m == 34) stat_fin(stA);
        if constexpr (m == 36 || m == 37) { nrm_pkt(xsA, hfA, stA, 2 * (m - 36)); nrm_pkt(xsA, hfA, stA, 2 * (m - 36) + 1); }
        if constexpr (k == 5 || m == 43) QSTAMP(4 + j);
        SB();
      });
    }
    STAMP(11);
    if (!more) break;
    ph += 8;
    unit = nunit;
    first = false;
  }
}

extern "C" void kernel_launch(void* const* d_in, const int* in_sizes, int n_in, void* d_out, int out_size,
                              void* d_ws, size_t ws_size, hipStream_t stream) {
  (void)hipFuncSetAttribute((const void*)swin_kernel, hipFuncAttributeMaxDynamicSharedMemorySize, LDS_TOTAL);
  swin_kernel<<<NWG, 512, LDS_TOTAL, stream>>>((const char*)d_in[0], (const char*)d_in[1], (const char*)d_in[2],
                                               (const char*)d_in[3], (const char*)d_in[4], (char*)d_out
  );
}
```

```cpp
#include <hip/hip_runtime.h>
#include <hip/hip_bf16.h>
#include <stdint.h>

using bf16x8 = __attribute__((ext_vector_type(8))) short;
using f32x16 = __attribute__((ext_vector_type(16))) float;
using u32x4 = __attribute__((ext_vector_type(4))) unsigned;
using f32x2 = __attribute__((ext_vector_type(2))) float;
using bf2 = __attribute__((ext_vector_type(2))) __bf16;
#if defined(__HIP_DEVICE_COMPILE__)
#define NOPK __attribute__((target("no-packed-fp32-ops")))
#else
#define NOPK
#endif
#define DI __device__ __forceinline__ NOPK
#define MFMA(a, b, c) __builtin_amdgcn_mfma_f32_32x32x16_bf16(__builtin_bit_cast(bf16x8, (a)), __builtin_bit_cast(bf16x8, (b)), (c), 0, 0, 0)
#define SB() __builtin_amdgcn_sched_barrier(0)
#define PIN(x) asm volatile("" : "+v"(x))
#define STORE_AUX 16

constexpr int CH = 96, HH = 224, WW = 224;
constexpr int ROWB = CH * 2;
constexpr int FRAMEB = HH * WW * ROWB;
constexpr int IMGROWB = WW * ROWB;
constexpr int NWIN = 28;
constexpr int NUNITS = NWIN * NWIN;
constexpr int WMAT_BYTES = 18 * 1024;
constexpr float LN_EPS = 1e-5f;
constexpr int NWG = 256;
constexpr float QK_SCALE = 0.10206207261596575f * 1.4426950408889634f;

template <int I> struct IC { static constexpr int v = I; };
template <int B, int E, class F> DI void sfor(F&& f) { if constexpr (B < E) { f(IC<B>{}); sfor<B + 1, E>(f); } }
#define LAM(M) [&](auto M) __attribute__((always_inline)) NOPK

DI unsigned pk2(float a, float b) {
  f32x2 v = {a, b};
  return __builtin_bit_cast(unsigned, __builtin_convertvector(v, bf2));
}
DI float lo16(unsigned u) { return __builtin_bit_cast(float, u << 16); }
DI float hi16(unsigned u) { return __builtin_bit_cast(float, u & 0xffff0000u); }
DI unsigned fbits(float f) { return __builtin_bit_cast(unsigned, f); }
DI float bitsf(unsigned u) { return __builtin_bit_cast(float, u); }
DI float xhalf_sum(float v) {
  auto r = __builtin_amdgcn_permlane32_swap(fbits(v), fbits(v), false, false);
  return bitsf(r[0]) + bitsf(r[1]);
}
DI float xhalf_max(float v) {
  auto r = __builtin_amdgcn_permlane32_swap(fbits(v), fbits(v), false, false);
  return __builtin_fmaxf(bitsf(r[0]), bitsf(r[1]));
}
DI float bsel(unsigned m, float a, float b) { return bitsf((m & fbits(a)) | (~m & fbits(b))); }
DI float max3(float a, float b, float c) { return __builtin_fmaxf(__builtin_fmaxf(a, b), c); }
DI u32x4 pack8(const f32x16& a, int s) {
  u32x4 r;
  r[0] = pk2(a[8 * s + 0], a[8 * s + 1]);
  r[1] = pk2(a[8 * s + 2], a[8 * s + 3]);
  r[2] = pk2(a[8 * s + 4], a[8 * s + 5]);
  r[3] = pk2(a[8 * s + 6], a[8 * s + 7]);
  return r;
}

typedef __attribute__((address_space(3))) char lds_char;
typedef __attribute__((address_space(3))) unsigned lds_u32;
DI u32x4 lds_ld(const lds_char* p) { return *(const __attribute__((address_space(3))) u32x4*)p; }
DI void lds_st(lds_char* p, u32x4 v) { *(__attribute__((address_space(3))) u32x4*)p = v; }

constexpr int LDS_CNT = 0;
DI void cnt_arrive(const lds_char* l16, unsigned dvoff, int k) {
  asm volatile("s_lshl_b64 exec, 1, %2\n\tds_add_u32 %0, %1 offset:%3\n\ts_mov_b64 exec, -1"
               :: "v"((unsigned)(size_t)l16), "v"(dvoff), "s"(k), "i"(LDS_CNT) : "memory", "scc");
}
DI unsigned cnt_peek(const lds_char* l16) {
  return __hip_atomic_load((const lds_u32*)(l16 + LDS_CNT), __ATOMIC_RELAXED, __HIP_MEMORY_SCOPE_WORKGROUP);
}
DI void cnt_wait(const lds_char* l16, int k, unsigned n, unsigned peeked) {
  const unsigned target = n * 16u * (unsigned)k;
  asm volatile("" ::: "memory");
  if (__builtin_expect((unsigned)__builtin_amdgcn_readlane(peeked, k) < target, 0)) {
    for (;;) {
      __builtin_amdgcn_s_sleep(1);
      if ((unsigned)__builtin_amdgcn_readlane(cnt_peek(l16), k) >= target) break;
    }
  }
  asm volatile("" ::: "memory");
}
DI void cnt_wait(const lds_char* l16, int k, unsigned n) { cnt_wait(l16, k, n, cnt_peek(l16)); }

typedef __attribute__((address_space(1))) unsigned gu32;
constexpr int SCR_OFF = 65536;
constexpr int XFS = 1552;
constexpr int XREG = 6304;
constexpr int OSTG = 4096;
constexpr int LDS_ID = 256;
constexpr int LDS_W0 = LDS_ID + 2048;
constexpr int LDS_X = LDS_W0 + 2 * WMAT_BYTES;
constexpr int LDS_W2 = LDS_X + 8 * XREG;
constexpr int LDS_O = LDS_W2 + 2 * WMAT_BYTES;
constexpr int LDS_RAW = LDS_W2;
constexpr int LDS_RAW_END = LDS_RAW + 73728;
constexpr int LDS_TOTAL = LDS_RAW_END;
static_assert(LDS_O + 8 * OSTG <= LDS_RAW_END && LDS_O + 2 * WMAT_BYTES <= LDS_RAW_END, "staging");
static_assert(LDS_TOTAL <= 163840, "lds");

DI void dma_tile(const char* row, unsigned voff, unsigned lds) {
  const char* r1 = row + FRAMEB; const char* r2 = row + 2 * (size_t)FRAMEB; const char* r3 = row + 3 * (size_t)FRAMEB;
  unsigned keep;
  asm volatile(
      "s_mov_b32 %0, m0\n\t"
      "s_mov_b32 m0, %6\n\ts_nop 0\n\t"
      "global_load_lds_dwordx4 %1, %2 nt\n\t"
      "s_add_u32 m0, m0, %7\n\ts_nop 0\n\t"
      "global_load_lds_dwordx4 %1, %3 nt\n\t"
      "s_add_u32 m0, m0, %7\n\ts_nop 0\n\t"
      "global_load_lds_dwordx4 %1, %4 nt\n\t"
      "s_add_u32 m0, m0, %7\n\ts_nop 0\n\t"
      "global_load_lds_dwordx4 %1, %5 nt\n\t"
      "s_mov_b32 exec_hi, 0\n\t"
      "global_load_lds_dwordx4 %1, %5 offset:1024 nt\n\t"
      "s_sub_u32 m0, m0, %7\n\ts_nop 0\n\t"
      "global_load_lds_dwordx4 %1, %4 offset:1024 nt\n\t"
      "s_sub_u32 m0, m0, %7\n\ts_nop 0\n\t"
      "global_load_lds_dwordx4 %1, %3 offset:1024 nt\n\t"
      "s_sub_u32 m0, m0, %7\n\ts_nop 0\n\t"
      "global_load_lds_dwordx4 %1, %2 offset:1024 nt\n\t"
      "s_mov_b32 exec_hi, -1\n\t"
      "s_mov_b32 m0, %0"
      : "=&s"(keep) : "v"(voff), "s"(row), "s"(r1), "s"(r2), "s"(r3), "s"(lds), "i"(XFS) : "memory", "scc");
}
DI void dma_tile_sc1(const char* row, unsigned voff, unsigned lds) {
  const char* r1 = row + FRAMEB; const char* r2 = row + 2 * (size_t)FRAMEB; const char* r3 = row + 3 * (size_t)FRAMEB;
  unsigned keep;
  asm volatile(
      "s_mov_b32 %0, m0\n\t"
      "s_mov_b32 m0, %6\n\ts_nop 0\n\t"
      "global_load_lds_dwordx4 %1, %2 sc1\n\t"
      "s_add_u32 m0, m0, %7\n\ts_nop 0\n\t"
      "global_load_lds_dwordx4 %1, %3 sc1\n\t"
      "s_add_u32 m0, m0, %7\n\ts_nop 0\n\t"
      "global_load_lds_dwordx4 %1, %4 sc1\n\t"
      "s_add_u32 m0, m0, %7\n\ts_nop 0\n\t"
      "global_load_lds_dwordx4 %1, %5 sc1\n\t"
      "s_mov_b32 exec_hi, 0\n\t"
      "global_load_lds_dwordx4 %1, %5 offset:1024 sc1\n\t"
      "s_sub_u32 m0, m0, %7\n\ts_nop 0\n\t"
      "global_load_lds_dwordx4 %1, %4 offset:1024 sc1\n\t"
      "s_sub_u32 m0, m0, %7\n\ts_nop 0\n\t"
      "global_load_lds_dwordx4 %1, %3 offset:1024 sc1\n\t"
      "s_sub_u32 m0, m0, %7\n\ts_nop 0\n\t"
      "global_load_lds_dwordx4 %1, %2 offset:1024 sc1\n\t"
      "s_mov_b32 exec_hi, -1\n\t"
      "s_mov_b32 m0, %0"
      : "=&s"(keep) : "v"(voff), "s"(row), "s"(r1), "s"(r2), "s"(r3), "s"(lds), "i"(XFS) : "memory", "scc");
}
DI void dma_piece(const char* base, unsigned voff, unsigned lds) {
  unsigned keep;
  asm volatile("s_mov_b32 %0, m0\n\ts_mov_b32 m0, %3\n\ts_nop 0\n\tglobal_load_lds_dwordx4 %1, %2\n\ts_mov_b32 m0, %0"
               : "=&s"(keep) : "v"(voff), "s"(base), "s"(lds) : "memory");
}
#define VMWAIT(n) asm volatile("s_waitcnt vmcnt(" #n ")" ::: "memory")
#define LGWAIT() asm volatile("s_waitcnt lgkmcnt(0)" ::: "memory")

#define STAMP(i) do {} while (0)
#define PSTAMP(i) do {} while (0)
#define QSTAMP(i) do {} while (0)

template <int CPR, int SS>
DI void raw_piece(const char* src, int piece, int lane, unsigned lds_img) {
  const unsigned q = (unsigned)(piece * 64 + lane);
  const unsigned row = (q * (unsigned)(((1 << 20) + CPR - 1) / CPR)) >> 20;
  const unsigned off = (SS == CPR * 16) ? 16u * (q ^ ((row >> 2) & 3u)) : row * (unsigned)(SS - CPR * 16) + 16u * (q ^ ((row >> 2) & 3u));
  dma_piece(src, off, lds_img + piece * 1024);
}
typedef short s16x4 __attribute__((ext_vector_type(4)));
DI void fold_read_rows(u32x4* d, const lds_char* img, int rb, int row0, int sw, int hh) {
  const int x = (sw >> 2) & 3;
  const lds_char* b0 = img + (row0 + sw) * rb + 16 * (hh ^ x);
  const lds_char* b1 = img + (row0 + sw) * rb + 16 * ((2 + hh) ^ x);
#pragma unroll
  for (int s = 0; s < 6; ++s) d[s] = lds_ld(((s & 1) ? b1 : b0) + 64 * (s >> 1));
}
DI void fold_read_cols(u32x4* d, const lds_char* img, int T, int lane) {
  const int hh = lane >> 5, cb = (lane >> 4) & 1, qq = (lane >> 2) & 3, pp = lane & 3;
  const int cc = 4 * T + 2 * cb + (pp & 1);
  const lds_char* b0 = img + (8 * hh + qq) * 192 + 16 * (cc ^ ((2 * hh) & 3)) + 8 * (pp >> 1);
  const lds_char* b1 = img + (8 * hh + qq + 4) * 192 + 16 * (cc ^ ((2 * hh + 1) & 3)) + 8 * (pp >> 1);
#pragma unroll
  for (int s = 0; s < 6; ++s) {
    s16x4 lo = __builtin_amdgcn_ds_read_tr16_b64_v4i16((__attribute__((address_space(3))) s16x4*)(b0 + s * 16 * 192));
    s16x4 hi = __builtin_amdgcn_ds_read_tr16_b64_v4i16((__attribute__((address_space(3))) s16x4*)(b1 + s * 16 * 192));
    d[s] = __builtin_bit_cast(u32x4, __builtin_shufflevector(lo, hi, 0, 1, 2, 3, 4, 5, 6, 7));
  }
}
DI void fold_compute(const u32x4 (&a)[18], const u32x4 (&b)[6], float sc, u32x4 (&res)[6]) {
  const f32x16 zero = {};
  f32x16 acc[3];
#pragma unroll
  for (int s = 0; s < 6; ++s)
#pragma unroll
    for (int Tin = 0; Tin < 3; ++Tin) acc[Tin] = MFMA(a[Tin * 6 + s], b[s], s == 0 ? zero : acc[Tin]);
#pragma unroll
  for (int Tin = 0; Tin < 3; ++Tin) {
#pragma unroll
    for (int i = 0; i < 16; ++i) acc[Tin][i] *= sc;
    res[2 * Tin] = pack8(acc[Tin], 0);
    res[2 * Tin + 1] = pack8(acc[Tin], 1);
  }
}
DI void fold_store(lds_char* wdst, const u32x4 (&res)[6]) {
#pragma unroll
  for (int s = 0; s < 6; ++s) lds_st(wdst + s * 1024, res[s]);
}

struct Stats { float s0, s1, q0, q1, rs, nb; };
DI void stat_pkt(const u32x4 (&xf)[6], f32x16 (&xs)[3], Stats& st, int k) {
  const int s = k >> 2, i = k & 3, T = s >> 1, e = 8 * (s & 1) + 2 * i;
  const float a = lo16(xf[s][i]), b = hi16(xf[s][i]);
  xs[T][e] = a; xs[T][e + 1] = b;
  if (k == 0) { st.s0 = a; st.s1 = b; st.q0 = a * a; st.q1 = b * b; }
  else { st.s0 += a; st.s1 += b; st.q0 = __builtin_fmaf(a, a, st.q0); st.q1 = __builtin_fmaf(b, b, st.q1); }
  PIN(st.s0); PIN(st.s1); PIN(st.q0); PIN(st.q1);
}
DI void stat_fin(Stats& st) {
  float s = xhalf_sum(st.s0 + st.s1), q = xhalf_sum(st.q0 + st.q1);
  float m = s * (1.f / CH);
  float var = __builtin_fmaxf(__builtin_fmaf(-m, m, q * (1.f / CH)), 0.f);
  st.rs = __builtin_amdgcn_rsqf(var + LN_EPS);
  st.nb = -m * st.rs;
  PIN(st.rs); PIN(st.nb);
}
DI void nrm_pkt(const f32x16 (&xs)[3], u32x4 (&hf)[6], const Stats& st, int k) {
  const int s = k >> 2, i = k & 3, T = s >> 1, e = 8 * (s & 1) + 2 * i;
  unsigned v = pk2(__builtin_fmaf(xs[T][e], st.rs, st.nb), __builtin_fmaf(xs[T][e + 1], st.rs, st.nb));
  PIN(v);
  hf[s][i] = v;
}
DI void cvt4(u32x4& d, const f32x16& a, int s) { d = pack8(a, s); PIN(d); }

constexpr int RD = 4;
constexpr int NWSEQ = 144;
DI constexpr int woff(int n) {
  n %= NWSEQ;
  if (n < 72) {
    int k = n % 36;
    if (k < 12) return LDS_W0 + (k & 1) * WMAT_BYTES + (k >> 1) * 1024;
    if (k < 24) return LDS_W0 + (6 + (k - 12)) * 1024;
    return LDS_W0 + WMAT_BYTES + (6 + (k - 24)) * 1024;
  }
  if (n < 108) return LDS_W2 + ((n - 72) % 18) * 1024;
  return LDS_W2 + WMAT_BYTES + (((n - 108) / 12) * 6 + (n - 108) % 6) * 1024;
}
constexpr int WB_BASE = LDS_W2;
struct Ctx {
  const lds_char* wa;
  const lds_char* wb;
  bool ms0, ms1, mg[4];
};
DI u32x4 wload(const Ctx& c, int n) {
  const int o = woff(n);
  return o < LDS_W2 ? lds_ld(c.wa + o) : lds_ld(c.wb + (o - WB_BASE));
}
#define WUSE(n) ring[(n) % RD]
#define WNEXT(n) ring[(n) % RD] = wload(c, (n) + RD)

struct Slot { int ty, T, s; };
DI constexpr Slot tslot(int m) {
  if (m < 12) return {m & 1, 0, m >> 1};
  if (m < 15) return {0, 1, m - 12};
  if (m < 17) return {2, 0, m - 15};
  if (m < 20) return {0, 1, m - 14};
  if (m < 23) return {0, 2, m - 20};
  if (m < 25) return {2, 1, m - 23};
  if (m < 28) return {0, 2, m - 22};
  if (m < 31) return {1, 1, m - 28};
  if (m < 33) return {2, 2, m - 31};
  if (m < 36) return {1, 1, m - 30};
  if (m < 42) return {1, 2, m - 36};
  return {3, (m - 42) >> 1, (m - 42) & 1};
}
DI constexpr int twidx(int m) {
  return m < 15 ? m : (m < 17 ? 15 : (m < 23 ? m - 2 : (m < 25 ? 21 : (m < 31 ? m - 4 : (m < 33 ? 27 : (m < 42 ? m - 6 : 36))))));
}
DI constexpr int sched_cnt(int m, int first, int last, int total) {
  return m <= first ? 0 : (m >= last ? total : (m - first) * total / (last - first));
}

struct TState { f32x16 g[3], v[3], sa; u32x4 hf[6], gp[2], vp[3][2], pf[2]; float e[4]; unsigned pk0, pk1; };
template <int K0, int K1>
DI void stat_pkts(const u32x4 (&xf)[6], f32x16 (&xs)[3], Stats& st) {
  if constexpr (K0 < K1) { stat_pkt(xf, xs, st, K0); stat_pkts<K0 + 1, K1>(xf, xs, st); }
}
template <int N0, int m, int M1, int HS, bool PE, bool OE>
DI void temporal_slots(const Ctx& c, u32x4 (&ring)[RD], TState& t, f32x16 (&xs)[3], const Stats& st,
                       const u32x4 (&nx)[6], f32x16 (&nxs)[3], Stats& nst, u32x4 (&nhf)[6], const f32x16 (&pxs)[3], lds_char* xw) {
  const f32x16 zero = {};
  if constexpr (m < M1) {
    constexpr Slot sl = tslot(m);
    constexpr int n = N0 + twidx(m);
    if constexpr (sl.ty == 0) { t.g[sl.T] = MFMA(WUSE(n), t.hf[sl.s], sl.s == 0 ? zero : t.g[sl.T]); WNEXT(n); }
    else if constexpr (sl.ty == 1) { t.v[sl.T] = MFMA(t.hf[sl.s], WUSE(n), sl.s == 0 ? zero : t.v[sl.T]); WNEXT(n); }
    else if constexpr (sl.ty == 2) t.sa = MFMA(t.hf[2 * sl.T + sl.s], t.gp[sl.s], (sl.T == 0 && sl.s == 0) ? zero : t.sa);
    else xs[sl.T] = MFMA(t.vp[sl.T][sl.s], t.pf[sl.s], xs[sl.T]);
    SB();
    if constexpr (m < 10) { nrm_pkt(xs, t.hf, st, 4 + 2 * m); nrm_pkt(xs, t.hf, st, 5 + 2 * m); }
    if constexpr (PE && m >= 2 && m < 8) { u32x4 a; cvt4(a, pxs[(m - 2) >> 1], m & 1); lds_st(xw + (m - 2) * 1024, a); }
    if constexpr (m == 12) cvt4(t.gp[0], t.g[0], 0);
    if constexpr (m == 13) cvt4(t.gp[1], t.g[0], 1);
    if constexpr (m == 14) cvt4(t.vp[0][0], t.v[0], 0);
    if constexpr (m == 15) cvt4(t.vp[0][1], t.v[0], 1);
    if constexpr (m == 21) { cvt4(t.gp[0], t.g[1], 0); }
    if constexpr (m == 22) { cvt4(t.gp[1], t.g[1], 1); }
    if constexpr (m == 29) { cvt4(t.gp[0], t.g[2], 0); }
    if constexpr (m == 30) { cvt4(t.gp[1], t.g[2], 1); }
    if constexpr (m == 35 || m == 36) {
#pragma unroll
      for (int k = 2 * (m - 35); k < 2 * (m - 35) + 2; ++k) {
        float s1 = t.sa[4 + k], s3 = t.sa[12 + k];
        asm("" : "+v"(s1)); asm("" : "+v"(s3));
        float a = c.ms0 ? s1 : t.sa[k];
        float b = c.ms0 ? s3 : t.sa[8 + k];
        t.e[k] = c.ms1 ? b : a; PIN(t.e[k]);
      }
    }
    if constexpr (m == 37) {
      float mx = max3(max3(t.e[0], t.e[1], t.e[2]), t.e[3], t.e[3]);
#pragma unroll
      for (int k = 0; k < 4; ++k) { t.e[k] = __builtin_amdgcn_exp2f(t.e[k] - mx); PIN(t.e[k]); }
    }
    if constexpr (m == 38) {
      float rinv = __builtin_amdgcn_rcpf((t.e[0] + t.e[1]) + (t.e[2] + t.e[3]));
      t.pk0 = pk2(t.e[0] * rinv, t.e[1] * rinv); t.pk1 = pk2(t.e[2] * rinv, t.e[3] * rinv);
      PIN(t.pk0); PIN(t.pk1);
    }
    if constexpr (m == 39) {
#pragma unroll
      for (int g = 0; g < 4; ++g) {
        t.pf[g >> 1][(g & 1) * 2 + 0] = c.mg[g] ? t.pk0 : 0u;
        t.pf[g >> 1][(g & 1) * 2 + 1] = c.mg[g] ? t.pk1 : 0u;
      }
      PIN(t.pf[0]); PIN(t.pf[1]);
    }
    if constexpr (m == 40) { cvt4(t.vp[1][0], t.v[1], 0); cvt4(t.vp[1][1], t.v[1], 1); }
    if constexpr (m == 44) { cvt4(t.vp[2][0], t.v[2], 0); cvt4(t.vp[2][1], t.v[2], 1); }
    if constexpr (m < 35) stat_pkts<sched_cnt(m, HS + 2, 35, 20), sched_cnt(m + 1, HS + 2, 35, 20)>(nx, nxs, nst);
    if constexpr (m == 41) stat_pkts<20, 22>(nx, nxs, nst);
    if constexpr (m == 42) stat_pkts<22, 24>(nx, nxs, nst);
    if constexpr (m == 43) stat_fin(nst);
    if constexpr (m == 45) { nrm_pkt(nxs, nhf, nst, 0); nrm_pkt(nxs, nhf, nst, 1); }
    if constexpr (m == 46) { nrm_pkt(nxs, nhf, nst, 2); nrm_pkt(nxs, nhf, nst, 3); }
    if constexpr (OE && m == 46) { u32x4 a; cvt4(a, xs[0], 0); lds_st(xw, a); }
    if constexpr (OE && m == 47) { u32x4 a; cvt4(a, xs[0], 1); lds_st(xw + 1024, a); }
    SB();
    temporal_slots<N0, m + 1, M1, HS, PE, OE>(c, ring, t, xs, st, nx, nxs, nst, nhf, pxs, xw);
  }
}

struct SMState { float m0, m1, mx, l0, l1, rinv; };
template <int u>
DI void sm_unit(f32x16& a, f32x16& b, SMState& s, u32x4 (&pf)[4]) {
  if constexpr (u < 4) {
    f32x16& x = (u < 2) ? a : b;
    const int o = 8 * (u & 1);
    if (u == 0) { s.m0 = max3(x[0], x[1], x[2]); s.m1 = max3(x[3], x[4], x[5]); s.m0 = max3(s.m0, x[6], x[7]); }
    else { s.m0 = max3(s.m0, x[o], x[o + 1]); s.m1 = max3(s.m1, x[o + 2], x[o + 3]); s.m0 = max3(s.m0, x[o + 4], x[o + 5]); s.m1 = max3(s.m1, x[o + 6], x[o + 7]); }
    PIN(s.m0); PIN(s.m1);
  } else if constexpr (u == 4) {
    s.mx = xhalf_max(__builtin_fmaxf(s.m0, s.m1)); PIN(s.mx);
  } else if constexpr (u < 21) {
    constexpr int k = u - 5;
    f32x16& x = (k < 8) ? a : b;
    constexpr int o = 2 * (k & 7);
    float e0 = __builtin_amdgcn_exp2f(x[o] - s.mx), e1 = __builtin_amdgcn_exp2f(x[o + 1] - s.mx);
    PIN(e0); PIN(e1);
    x[o] = e0; x[o + 1] = e1;
    if constexpr (k >= 2) {
      f32x16& y = (k - 1 < 8) ? a : b;
      constexpr int o1 = 2 * ((k - 1) & 7);
      if constexpr (k == 2) { s.l0 = a[0] + y[o1]; s.l1 = a[1] + y[o1 + 1]; } else { s.l0 += y[o1]; s.l1 += y[o1 + 1]; }
      PIN(s.l0); PIN(s.l1);
    }
  } else if constexpr (u == 21) {
    s.rinv = __builtin_amdgcn_rcpf(xhalf_sum((s.l0 + b[14]) + (s.l1 + b[15]))); PIN(s.rinv);
  } else {
    const int k = u - 22;
    f32x16& x = (k < 4) ? a : b;
    const int o = 4 * (k & 3);
    unsigned p0 = pk2(x[o] * s.rinv, x[o + 1] * s.rinv), p1 = pk2(x[o + 2] * s.rinv, x[o + 3] * s.rinv);
    PIN(p0); PIN(p1);
    pf[k >> 1][2 * (k & 1)] = p0; pf[k >> 1][2 * (k & 1) + 1] = p1;
  }
}

__global__ __launch_bounds__(512, 2) NOPK void swin_kernel(const char* __restrict__ x, const char* __restrict__ wqkv_t,
                                                           const char* __restrict__ wo_t, const char* __restrict__ wqkv_s,
                                                           const char* __restrict__ wo_s, char* __restrict__ out,
                                                           char* __restrict__ ws) {
  extern __shared__ __attribute__((aligned(16))) char smem_[];
  lds_char* smem = (lds_char*)smem_;
  const unsigned smem_a = (unsigned)(size_t)smem;
  const int tid = threadIdx.x, lane = tid & 63;
  const int wid8 = __builtin_amdgcn_readfirstlane(tid >> 6);
  const int grp = wid8 >> 2, wid = wid8 & 3;
  const int r = lane & 31, hh = lane >> 5;
  const int p = r >> 2, f = r & 3;
  const int sw = (r & 0x13) | ((r & 4) << 1) | ((r & 8) >> 1);
  const unsigned dvoff = lane * 16;
  const unsigned xr_a = smem_a + LDS_X + wid8 * XREG;

  constexpr int FULL = 0, TONLY = 1, SONLY = 2, NONE = 3;
  const int bid = blockIdx.x;
  const int xj = bid >> 3, ub = (bid & 7) * 98;
  const int lunit = ub + 96 + (xj & 1), hslot = ((bid & 7) * 2 + (xj & 1)) * 4 + wid;
  int mode = FULL, unit = ub + xj + 32 * grp, nmode = grp == 0 ? FULL : NONE, nunit = ub + xj + 64;
  if (grp == 0 && xj < 2) { mode = TONLY; unit = lunit; nunit = ub + xj; }
  const int lastmode = (grp == 0 && (xj == 2 || xj == 3)) ? SONLY : NONE;
  STAMP(0);
  gu32* hfl = (gu32*)(ws + hslot * 256);
  unsigned hev;
  asm volatile("global_load_dword %0, %1, %2 sc1" : "=v"(hev) : "v"(0u), "s"(hfl) : "memory");
  const lds_char* l16 = smem + lane * 16;
  constexpr int C_TW = 5, C_MS = 6, C_RAW = 7, C_VS = 8, C_QK = 9;
  constexpr int IMG = WMAT_BYTES;
  {
    const int rot = (blockIdx.x * 11) % 72;
#pragma unroll
    for (int i = 0; i < 9; ++i) {
      int pc = wid8 * 9 + i + rot; pc -= pc >= 72 ? 72 : 0;
      if (pc < 54) raw_piece<36, 576>(wqkv_t, pc, lane, smem_a + LDS_RAW);
      else raw_piece<12, 192>(wo_t, pc - 54, lane, smem_a + LDS_RAW + 3 * IMG);
    }
#pragma unroll
    for (int i = 0; i < 5; ++i) {
      int pc = (wid8 * 5 + i + rot) % 36;
      raw_piece<24, 576>(wqkv_s, pc, lane, smem_a + LDS_W0);
    }
    if (tid < 64) ((lds_u32*)(smem + LDS_CNT))[tid] = 0u;
    asm volatile("s_barrier" ::: "memory");
    {
      const int wy = unit / NWIN, wx = unit % NWIN;
      dma_tile(x + (size_t)(wy * 8 + 2 * wid) * IMGROWB + (size_t)(wx * 8) * ROWB, dvoff, xr_a);
    }
    PSTAMP(0);
    if (wid8 == 7) {
      u32x4 idf[2];
#pragma unroll
      for (int s = 0; s < 2; ++s)
#pragma unroll
        for (int d = 0; d < 4; ++d)
          idf[s][d] = (s == (sw >> 4) && hh == ((sw >> 3) & 1) && d == ((sw & 7) >> 1)) ? (0x3F80u << (16 * (sw & 1))) : 0u;
      lds_st(smem + LDS_ID + lane * 16, idf[0]); lds_st(smem + LDS_ID + 1024 + lane * 16, idf[1]);
    }
    asm volatile("s_waitcnt vmcnt(13) lgkmcnt(0)\n\ts_barrier" ::: "memory");
    PSTAMP(1);
    u32x4 fa[18], fb[6], res[6];
    if (wid8 < 6) {
      const int T1 = wid8 % 3;
      const lds_char* aimg = smem + LDS_RAW + (wid8 < 3 ? 384 : 0);
#pragma unroll
      for (int Tin = 0; Tin < 3; ++Tin) fold_read_rows(fa + 6 * Tin, aimg, 576, 32 * Tin, sw, hh);
      if (wid8 < 3) fold_read_cols(fb, smem + LDS_RAW + 3 * IMG, T1, lane);
      else fold_read_rows(fb, smem + LDS_RAW + 192, 576, 32 * T1, sw, hh);
      asm volatile("s_waitcnt vmcnt(8) lgkmcnt(0)\n\ts_barrier" ::: "memory");
      PSTAMP(2);
      fold_compute(fa, fb, wid8 < 3 ? 1.f : QK_SCALE, res);
      cnt_wait(l16, C_QK, 2);
      fold_store(smem + LDS_W0 + (wid8 < 3 ? IMG : 0) + T1 * 6144 + lane * 16, res);
      cnt_arrive(l16, dvoff, C_TW);
    } else {
      asm volatile("s_waitcnt vmcnt(8) lgkmcnt(0)\n\ts_barrier" ::: "memory");
      PSTAMP(2);
      u32x4 fb2[6];
      const int T1 = wid8 == 6 ? 0 : 2;
#pragma unroll
      for (int Tin = 0; Tin < 3; ++Tin) fold_read_rows(fa + 6 * Tin, smem + LDS_W0, 384, 32 * Tin, sw, hh);
      fold_read_rows(fb, smem + LDS_W0 + 192, 384, 32 * T1, sw, hh);
      fold_read_rows(fb2, smem + LDS_W0 + 192, 384, 32, sw, hh);
      cnt_arrive(l16, dvoff, C_QK);
#pragma unroll 3
      for (int i = 0; i < 18; ++i) {
        if (wid8 == 6) raw_piece<12, 576>(wqkv_s + 384, i, lane, smem_a + LDS_O);
        else raw_piece<12, 192>(wo_s, i, lane, smem_a + LDS_O + IMG);
      }
      fold_compute(fa, fb, QK_SCALE, res);
      fold_store(smem + LDS_W2 + T1 * 6144 + lane * 16, res);
      if (wid8 == 6) { fold_compute(fa, fb2, QK_SCALE, res); fold_store(smem + LDS_W2 + 6144 + lane * 16, res); }
      cnt_arrive(l16, dvoff, C_MS);
      VMWAIT(0);
      cnt_arrive(l16, dvoff, C_RAW);
    }
    PSTAMP(3);
  }

  const int cW = 1 + 2 * grp, cR = 2 + 2 * grp;
  auto laddr = [&](unsigned off) __attribute__((always_inline)) NOPK { off += smem_a; asm("" : "+v"(off)); return (lds_char*)(size_t)off; };
  const unsigned xg = LDS_X + grp * 4 * XREG;
  lds_char* xw = laddr(xg + f * XREG + (hh * 32 + 8 * wid + p) * 16);
  const lds_char* xr = laddr(xg + wid * XREG + lane * 16);
  const lds_char* xl = laddr(xg + wid * XREG + f * XFS + p * ROWB + hh * 16);
  lds_char* ow0 = laddr(LDS_O + wid8 * OSTG + r * 64 + 16 * (hh ^ ((r >> 1) & 3)));
  lds_char* ow1 = laddr(LDS_O + wid8 * OSTG + r * 64 + 16 * ((2 + hh) ^ ((r >> 1) & 3)));
  const lds_char* ord = laddr(LDS_O + wid8 * OSTG + lane * 16);
  const f32x16 zero = {};
  Ctx c;
  c.wa = l16; c.wb = laddr(WB_BASE + lane * 16);
  {
    c.ms0 = (p >> 1) & 1;
    c.ms1 = (p >> 2) & 1;
    const bool valid = (hh == (p & 1));
#pragma unroll
    for (int g = 0; g < 4; ++g) c.mg[g] = valid && (p >> 1) == g;
  }
  unsigned ooff = (lane >> 5) * 2 * IMGROWB + ((lane >> 2) & 7) * ROWB + 16 * ((lane & 3) ^ ((lane >> 3) & 3));
  asm("" : "+v"(ooff));
  auto orsrc = __builtin_amdgcn_make_buffer_rsrc((void*)out, 0, 4 * FRAMEB, 0x00020000);

  f32x16 xsA[3], xsB[3], xsC[3]; Stats stA, stB, stC; u32x4 hfA[6];
  u32x4 x1[2][6], h2[2][6];
  u32x4 ring[RD];
  bool first = true, cold = true;
  unsigned ph = 0, pk;
  const char* nxu; int nstep; bool more; unsigned hepoch;

  auto cold_start2 = [&]() __attribute__((always_inline)) NOPK {
    const int wy = unit / NWIN, wx = unit % NWIN;
    u32x4 xin[6];
    PSTAMP(7);
#pragma unroll
    for (int s = 0; s < 6; ++s) xin[s] = lds_ld(xl + 32 * s);
    LGWAIT();
    dma_tile(x + (size_t)(wy * 8 + 2 * wid + 1) * IMGROWB + (size_t)(wx * 8) * ROWB, dvoff, xr_a);
    stat_pkts<0, 24>(xin, xsA, stA);
    stat_fin(stA);
#pragma unroll
    for (int k = 0; k < 4; ++k) nrm_pkt(xsA, hfA, stA, k);
    PSTAMP(8);
    cnt_wait(l16, C_TW, 6);
#pragma unroll
    for (int n = 0; n < RD; ++n) ring[n] = wload(c, n);
  };
  auto next_setup = [&]() __attribute__((always_inline)) NOPK {
    more = nmode != NONE;
    if (nmode == SONLY) { nxu = ws + SCR_OFF + (size_t)(hslot * 2) * 1536; nstep = 1536; }
    else { const int ny = nunit / NWIN, nx = nunit % NWIN; nxu = x + (size_t)(ny * 8 + 2 * wid) * IMGROWB + (size_t)(nx * 8) * ROWB; nstep = IMGROWB; }
  };
  auto phaseT = [&]() __attribute__((always_inline)) NOPK {
    STAMP(1);
    TState t2;
    {
      TState t; u32x4 xin[6];
      t.hf[0] = hfA[0];
      temporal_slots<0, 0, 11, 10, false, false>(c, ring, t, xsA, stA, xin, xsB, stB, t2.hf, xsA, xw);
      if (cold) { VMWAIT(0); } else { VMWAIT(12); }
#pragma unroll
      for (int s = 0; s < 6; ++s) xin[s] = lds_ld(xl + 32 * s);
      cnt_arrive(l16, dvoff, cR);
      temporal_slots<0, 11, 46, 10, false, false>(c, ring, t, xsA, stA, xin, xsB, stB, t2.hf, xsA, xw);
      pk = cnt_peek(l16);
      temporal_slots<0, 46, 48, 10, false, false>(c, ring, t, xsA, stA, xin, xsB, stB, t2.hf, xsA, xw);
    }
    STAMP(2);
    if (first && grp == 1 && wid != 2) {
      const int T2 = wid == 3 ? 2 : wid;
      cnt_wait(l16, C_RAW, 2);
      u32x4 fb[6];
      unsigned d16 = dvoff; asm volatile("" : "+v"(d16));
      const int ln = d16 >> 4, r2 = ln & 31, sw2 = (r2 & 0x13) | ((r2 & 4) << 1) | ((r2 & 8) >> 1);
      fold_read_cols(fb, smem + LDS_O + IMG, T2, ln);
#pragma unroll
      for (int Tin = 0; Tin < 3; ++Tin) {
        u32x4 fa[6];
        fold_read_rows(fa, smem + LDS_O, 192, 32 * Tin, sw2, ln >> 5);
        f32x16 acc = zero;
#pragma unroll
        for (int s = 0; s < 6; ++s) acc = MFMA(fa[s], fb[s], acc);
        lds_char* wdst = smem + LDS_W2 + IMG + T2 * 6144 + Tin * 2048 + d16;
        lds_st(wdst, pack8(acc, 0)); lds_st(wdst + 1024, pack8(acc, 1));
      }
      cnt_arrive(l16, dvoff, C_VS);
    }
    {
      temporal_slots<36, 0, 2, 19, true, true>(c, ring, t2, xsB, stB, x1[0], xsC, stC, h2[0], xsA, xw);
      cnt_wait(l16, cR, ph + 4, pk);
      temporal_slots<36, 2, 8, 19, true, true>(c, ring, t2, xsB, stB, x1[0], xsC, stC, h2[0], xsA, xw);
      cnt_arrive(l16, dvoff, cW);
      temporal_slots<36, 8, 17, 19, true, true>(c, ring, t2, xsB, stB, x1[0], xsC, stC, h2[0], xsA, xw);
      pk = cnt_peek(l16);
      temporal_slots<36, 17, 20, 19, true, true>(c, ring, t2, xsB, stB, x1[0], xsC, stC, h2[0], xsA, xw);
      STAMP(3);
      cnt_wait(l16, cW, ph + 4, pk);
#pragma unroll
      for (int s = 0; s < 6; ++s) x1[0][s] = lds_ld(xr + s * 1024);
      cnt_arrive(l16, dvoff, cR);
      if (first) cnt_wait(l16, C_MS, 2);
      STAMP(4);
      temporal_slots<36, 20, 43, 19, true, true>(c, ring, t2, xsB, stB, x1[0], xsC, stC, h2[0], xsA, xw);
      pk = cnt_peek(l16);
      temporal_slots<36, 43, 46, 19, true, true>(c, ring, t2, xsB, stB, x1[0], xsC, stC, h2[0], xsA, xw);
      cnt_wait(l16, cR, ph + 8, pk);
      temporal_slots<36, 46, 48, 19, true, true>(c, ring, t2, xsB, stB, x1[0], xsC, stC, h2[0], xsA, xw);
    }
    STAMP(5);
  };
  auto phaseS = [&](auto SOC) __attribute__((always_inline)) NOPK {
    constexpr bool SO = SOC.v != 0;
    const int wy = unit / NWIN, wx = unit % NWIN;
    f32x16 sa[2][2]; u32x4 gq[6];
    {
      f32x16 g[3], xsD[3]; Stats stD;
      sfor<0, 10>(LAM(M) {
        constexpr int m = M.v, n = 72 + m, T = m / 6, s = m % 6;
        g[T] = MFMA(WUSE(n), h2[0][s], s == 0 ? zero : g[T]); WNEXT(n);
        SB();
        if constexpr (m < 2) {
          if constexpr (!SO) {
            u32x4 a, b; cvt4(a, xsB[1 + m], 0); lds_st(xw + (2 + 2 * m) * 1024, a); cvt4(b, xsB[1 + m], 1); lds_st(xw + (3 + 2 * m) * 1024, b);
            if constexpr (m == 1) cnt_arrive(l16, dvoff, cW);
          }
        }
        if constexpr (m < 5) {
#pragma unroll
          for (int k = 4 + 4 * m; k < 8 + 4 * m; ++k) nrm_pkt(xsC, h2[0], stC, k);
        }
        if constexpr (m == 7) { cvt4(gq[0], g[0], 0); cvt4(gq[1], g[0], 1); }
        if constexpr (!SO && m == 8) pk = cnt_peek(l16);
        SB();
      });
      STAMP(6);
      if constexpr (!SO) {
        cnt_wait(l16, cW, ph + 8, pk);
#pragma unroll
        for (int s = 0; s < 6; ++s) x1[1][s] = lds_ld(xr + s * 1024);
      } else {
        VMWAIT(12);
#pragma unroll
        for (int s = 0; s < 6; ++s) x1[1][s] = lds_ld(xl + 32 * s);
      }
      STAMP(7);
      sfor<10, 24>(LAM(M) {
        constexpr int m = M.v;
        if constexpr (m < 12) { constexpr int n = 72 + m; g[1] = MFMA(WUSE(n), h2[0][m - 6], g[1]); WNEXT(n); }
        else if constexpr (m < 14) sa[0][0] = MFMA(h2[0][m - 12], gq[m - 12], m == 12 ? zero : sa[0][0]);
        else if constexpr (m < 20) { constexpr int n = 72 + m - 2; g[2] = MFMA(WUSE(n), h2[0][m - 14], m == 14 ? zero : g[2]); WNEXT(n); }
        else sa[0][0] = MFMA(h2[0][m - 18], gq[m - 18], sa[0][0]);
        SB();
        if constexpr (m == 13) {
          LGWAIT();
          if (!SO && nmode == SONLY) {
            for (int spin = 0; spin < (1 << 22); ++spin) {
              if (__builtin_amdgcn_readfirstlane(__hip_atomic_load(hfl + 32, __ATOMIC_RELAXED, __HIP_MEMORY_SCOPE_AGENT) - hepoch) == 1u) break;
              __builtin_amdgcn_s_sleep(8);
            }
            asm volatile("" ::: "memory");
            if (lane == 0) __hip_atomic_store(hfl, hepoch + 1u, __ATOMIC_RELAXED, __HIP_MEMORY_SCOPE_AGENT);
            dma_tile_sc1(nxu, dvoff, xr_a);
          } else if (!SO && more) dma_tile(nxu, dvoff, xr_a);
        }
        if constexpr (m == 14) { cvt4(gq[2], g[1], 0); cvt4(gq[3], g[1], 1); }
        if constexpr (m == 21) { cvt4(gq[4], g[2], 0); cvt4(gq[5], g[2], 1); }
        stat_pkts<sched_cnt(m, 11, 20, 24), sched_cnt(m + 1, 11, 20, 24)>(x1[1], xsD, stD);
        if constexpr (m == 19) stat_fin(stD);
        if constexpr (m >= 20) {
#pragma unroll
          for (int k = 6 * (m - 20); k < 6 * (m - 19); ++k) nrm_pkt(xsD, h2[1], stD, k);
        }
        SB();
      });
    }
    STAMP(8);
    u32x4 pf[2][4];
    if (!SO && ph < 16) cnt_wait(l16, C_VS, 3);
    {
      f32x16 g[3]; u32x4 gp[2][2]; SMState sm;
      sfor<0, 36>(LAM(M) {
        constexpr int m = M.v;
        if constexpr (m < 6) sa[0][1] = MFMA(h2[1][m], gq[m], m == 0 ? zero : sa[0][1]);
        else if constexpr (m < 18) { constexpr int n = 90 + m - 6, T = (m - 6) / 6, s = (m - 6) % 6; g[T] = MFMA(WUSE(n), h2[1][s], s == 0 ? zero : g[T]); WNEXT(n); }
        else if constexpr (m < 22) { constexpr int kt = (m - 18) & 1, s = (m - 18) >> 1; sa[1][kt] = MFMA(h2[kt][s], gp[0][s], s == 0 ? zero : sa[1][kt]); }
        else if constexpr (m < 28) { constexpr int n = 90 + m - 10, s = m - 22; g[2] = MFMA(WUSE(n), h2[1][s], s == 0 ? zero : g[2]); WNEXT(n); }
        else { constexpr int T = (m - 28) / 4 + 1, kt = (m - 28) & 1, s = ((m - 28) >> 1) & 1; sa[1][kt] = MFMA(h2[kt][2 * T + s], gp[T & 1][s], sa[1][kt]); }
        SB();
        if constexpr (m == 14) { cvt4(gp[0][0], g[0], 0); cvt4(gp[0][1], g[0], 1); }
        if constexpr (m == 20) { cvt4(gp[1][0], g[1], 0); cvt4(gp[1][1], g[1], 1); }
        if constexpr (m == 30) { cvt4(gp[0][0], g[2], 0); cvt4(gp[0][1], g[2], 1); }
        if constexpr (m == 2 || m == 3) sm_unit<m - 2>(sa[0][0], sa[0][1], sm, pf[0]);
        if constexpr (m >= 8) sm_unit<m - 6>(sa[0][0], sa[0][1], sm, pf[0]);
        SB();
      });
    }
    STAMP(9);
    u32x4 vp[3][4], idf[2]; f32x16 vlast;
    {
      f32x16 v[6]; SMState sm;
      sfor<0, 36>(LAM(M) {
        constexpr int m = M.v, ch = m / 6, s = m % 6, kt = ch & 1, n = 108 + m;
        v[ch] = MFMA(h2[kt][s], WUSE(n), s == 0 ? zero : v[ch]); WNEXT(n);
        SB();
        if constexpr (m >= 2 && m < 32) sm_unit<m - 2>(sa[1][0], sa[1][1], sm, pf[1]);
        if constexpr (ch > 0 && (s == 2 || s == 3)) cvt4(vp[(ch - 1) >> 1][2 * ((ch - 1) & 1) + s - 2], v[ch - 1], s - 2);
        if constexpr (m == 33) { idf[0] = lds_ld(c.wa + LDS_ID); idf[1] = lds_ld(c.wa + LDS_ID + 1024); }
        SB();
      });
      vlast = v[5];
    }
    STAMP(10);
    {
      u32x4 xin[6];
      QSTAMP(1);
      if (!SO && more) {
        VMWAIT(0);
        QSTAMP(2);
#pragma unroll
        for (int s = 0; s < 6; ++s) xin[s] = lds_ld(xl + 32 * s);
        LGWAIT();
        if (nmode == SONLY) dma_tile_sc1(nxu + nstep, dvoff, xr_a); else dma_tile(nxu + nstep, dvoff, xr_a);
      } else {
#pragma unroll
        for (int s = 0; s < 6; ++s) xin[s] = u32x4{};
      }
      const unsigned obase = wid * FRAMEB + (wy * 8) * IMGROWB + (wx * 8) * ROWB;
      f32x16 o[6]; u32x4 ov[2];
      QSTAMP(3);
      sfor<0, 44>(LAM(M) {
        constexpr int m = M.v, j = m / 6, k = m % 6;
        if constexpr (m < 36) {
          constexpr int q = j / 3, T = j % 3;
          if constexpr (k < 2) o[j] = MFMA(idf[k], x1[q][2 * T + k], k == 0 ? zero : o[j]);
          else o[j] = MFMA(vp[T][k - 2], pf[q][k - 2], o[j]);
          SB();
        }
        if constexpr (m == 2 || m == 3) cvt4(vp[2][m], vlast, m - 2);
        if constexpr (j >= 2 && k == 1) {
          constexpr int jj = j - 2, q = jj / 3, T = jj % 3;
          SB();
          __builtin_amdgcn_raw_buffer_store_b128(ov[0], orsrc, ooff, obase + q * IMGROWB + 64 * T, STORE_AUX);
          __builtin_amdgcn_raw_buffer_store_b128(ov[1], orsrc, ooff, obase + (4 + q) * IMGROWB + 64 * T, STORE_AUX);
          SB(); asm volatile("s_nop 1" ::: "memory"); SB();
        }
        if constexpr (j >= 1 && j <= 6) {
          constexpr int jj = j - 1;
          constexpr int ob = (jj & 1) * 2048;
          if constexpr (k == 2) { u32x4 a; cvt4(a, o[jj], 0); lds_st(ow0 + ob, a); }
          if constexpr (k == 3) { u32x4 a; cvt4(a, o[jj], 1); lds_st(ow1 + ob, a); asm volatile("" ::: "memory"); }
          if constexpr (k == 4) { ov[0] = lds_ld(ord + ob); ov[1] = lds_ld(ord + ob + 1024); asm volatile("" ::: "memory"); }
        }
        if constexpr (!SO) stat_pkts<sched_cnt(m, 2, 34, 24), sched_cnt(m + 1, 2, 34, 24)>(xin, xsA, stA);
        if constexpr (!SO && m == 34) stat_fin(stA);
        if constexpr (!SO && (m == 36 || m == 37)) { nrm_pkt(xsA, hfA, stA, 2 * (m - 36)); nrm_pkt(xsA, hfA, stA, 2 * (m - 36) + 1); }
        if constexpr (k == 5 || m == 43) QSTAMP(4 + j);
        SB();
      });
    }
    STAMP(11);
  };

  VMWAIT(0);
  asm volatile("" : "+v"(hev));
  hepoch = __builtin_amdgcn_readfirstlane(hev);
  if (mode == TONLY) {
    next_setup();
    cold_start2();
    phaseT();
#pragma unroll
    for (int s = 2; s < 6; ++s) lds_st(xw + s * 1024, pack8(xsB[s >> 1], s & 1));
    cnt_arrive(l16, dvoff, cW);
    cnt_wait(l16, cW, ph + 8);
#pragma unroll
    for (int s = 0; s < 6; ++s) x1[1][s] = lds_ld(xr + s * 1024);
    const int hidx = hslot;
    auto srs = __builtin_amdgcn_make_buffer_rsrc((void*)(ws + SCR_OFF), 0, 4 * FRAMEB, 0x00020000);
#pragma unroll
    for (int i = 0; i < 2; ++i) {
#pragma unroll
      for (int s = 0; s < 6; ++s) lds_st((lds_char*)xl + 32 * s, x1[i][s]);
      asm volatile("" ::: "memory");
#pragma unroll
      for (int k = 0; k < 6; ++k) {
        const unsigned q = k * 64 + lane, row = (q * 683u) >> 16, n = q - 96u * row;
        u32x4 v = lds_ld(smem + LDS_X + wid8 * XREG + row * XFS + 16 * n);
        __builtin_amdgcn_raw_buffer_store_b128(v, srs, row * FRAMEB + 16 * n + (hidx * 2 + i) * 1536, 0, 16);
      }
      LGWAIT();
    }
    PSTAMP(10);
    dma_tile(nxu, dvoff, xr_a);
    ph += 8; unit = nunit; nunit = ub + xj + 64; first = false;
    VMWAIT(0);
    PSTAMP(11);
    if (lane == 0) __hip_atomic_store(hfl + 32, hepoch + 1u, __ATOMIC_RELAXED, __HIP_MEMORY_SCOPE_AGENT);
  }
  cold_start2();
  for (;;) {
    next_setup();
    phaseT();
    phaseS(IC<0>{});
    if (nmode != FULL) break;
    ph += 8;
    unit = nunit; nmode = lastmode; nunit = lunit;
    first = false; cold = false;
  }
  if (nmode == SONLY) {
    unit = nunit;
#pragma unroll
    for (int T = 0; T < 3; ++T) xsC[T] = xsA[T];
    stC = stA; h2[0][0] = hfA[0];
#pragma unroll
    for (int s = 0; s < 6; ++s) x1[0][s] = pack8(xsA[s >> 1], s & 1);
#pragma unroll
    for (int n = 0; n < RD; ++n) ring[n] = wload(c, 72 + n);
    phaseS(IC<1>{});
  }
}

extern "C" void kernel_launch(void* const* d_in, const int* in_sizes, int n_in, void* d_out, int out_size,
                              void* d_ws, size_t ws_size, hipStream_t stream) {
  (void)hipFuncSetAttribute((const void*)swin_kernel, hipFuncAttributeMaxDynamicSharedMemorySize, LDS_TOTAL);
  swin_kernel<<<NWG, 512, LDS_TOTAL, stream>>>((const char*)d_in[0], (const char*)d_in[1], (const char*)d_in[2],
                                               (const char*)d_in[3], (const char*)d_in[4], (char*)d_out, (char*)d_ws);
}
```

```cpp
#include <hip/hip_runtime.h>
#include <hip/hip_bf16.h>
#include <stdint.h>

using bf16x8 = __attribute__((ext_vector_type(8))) short;
using f32x16 = __attribute__((ext_vector_type(16))) float;
using u32x4 = __attribute__((ext_vector_type(4))) unsigned;
using f32x2 = __attribute__((ext_vector_type(2))) float;
using bf2 = __attribute__((ext_vector_type(2))) __bf16;
#if defined(__HIP_DEVICE_COMPILE__)
#define NOPK __attribute__((target("no-packed-fp32-ops")))
#else
#define NOPK
#endif
#define DI __device__ __forceinline__ NOPK
#define MFMA(a, b, c) __builtin_amdgcn_mfma_f32_32x32x16_bf16(__builtin_bit_cast(bf16x8, (a)), __builtin_bit_cast(bf16x8, (b)), (c), 0, 0, 0)
#define SB() __builtin_amdgcn_sched_barrier(0)
#define PIN(x) asm volatile("" : "+v"(x))
#define STORE_AUX 16
#define CODEPF 32768

constexpr int CH = 96, HH = 224, WW = 224;
constexpr int ROWB = CH * 2;
constexpr int FRAMEB = HH * WW * ROWB;
constexpr int IMGROWB = WW * ROWB;
constexpr int NWIN = 28;
constexpr int NUNITS = NWIN * NWIN;
constexpr int WMAT_BYTES = 18 * 1024;
constexpr float LN_EPS = 1e-5f;
constexpr int NWG = 256;
constexpr float QK_SCALE = 0.10206207261596575f * 1.4426950408889634f;

template <int I> struct IC { static constexpr int v = I; };
template <int B, int E, class F> DI void sfor(F&& f) { if constexpr (B < E) { f(IC<B>{}); sfor<B + 1, E>(f); } }
#define LAM(M) [&](auto M) __attribute__((always_inline)) NOPK

DI unsigned pk2(float a, float b) {
  f32x2 v = {a, b};
  return __builtin_bit_cast(unsigned, __builtin_convertvector(v, bf2));
}
DI float lo16(unsigned u) { return __builtin_bit_cast(float, u << 16); }
DI float hi16(unsigned u) { return __builtin_bit_cast(float, u & 0xffff0000u); }
DI unsigned fbits(float f) { return __builtin_bit_cast(unsigned, f); }
DI float bitsf(unsigned u) { return __builtin_bit_cast(float, u); }
DI float xhalf_sum(float v) {
  auto r = __builtin_amdgcn_permlane32_swap(fbits(v), fbits(v), false, false);
  return bitsf(r[0]) + bitsf(r[1]);
}
DI float xhalf_max(float v) {
  auto r = __builtin_amdgcn_permlane32_swap(fbits(v), fbits(v), false, false);
  return __builtin_fmaxf(bitsf(r[0]), bitsf(r[1]));
}
DI float max3(float a, float b, float c) { return __builtin_fmaxf(__builtin_fmaxf(a, b), c); }
DI u32x4 pack8(const f32x16& a, int s) {
  u32x4 r;
  r[0] = pk2(a[8 * s + 0], a[8 * s + 1]);
  r[1] = pk2(a[8 * s + 2], a[8 * s + 3]);
  r[2] = pk2(a[8 * s + 4], a[8 * s + 5]);
  r[3] = pk2(a[8 * s + 6], a[8 * s + 7]);
  return r;
}

typedef __attribute__((address_space(3))) char lds_char;
typedef __attribute__((address_space(3))) unsigned lds_u32;
DI u32x4 lds_ld(const lds_char* p) { return *(const __attribute__((address_space(3))) u32x4*)p; }
DI void lds_st(lds_char* p, u32x4 v) { *(__attribute__((address_space(3))) u32x4*)p = v; }

constexpr int LDS_CNT = 0;
DI void cnt_arrive(const lds_char* l16, unsigned dvoff, int k) {
  asm volatile("s_lshl_b64 exec, 1, %2\n\tds_add_u32 %0, %1 offset:%3\n\ts_mov_b64 exec, -1"
               :: "v"((unsigned)(size_t)l16), "v"(dvoff), "s"(k), "i"(LDS_CNT) : "memory", "scc");
}
DI unsigned cnt_peek(const lds_char* l16) {
  return __hip_atomic_load((const lds_u32*)(l16 + LDS_CNT), __ATOMIC_RELAXED, __HIP_MEMORY_SCOPE_WORKGROUP);
}
DI void cnt_wait(const lds_char* l16, int k, unsigned n, unsigned peeked) {
  const unsigned target = n * 16u * (unsigned)k;
  asm volatile("" ::: "memory");
  if (__builtin_expect((unsigned)__builtin_amdgcn_readlane(peeked, k) < target, 0)) {
    for (;;) {
      __builtin_amdgcn_s_sleep(1);
      if ((unsigned)__builtin_amdgcn_readlane(cnt_peek(l16), k) >= target) break;
    }
  }
  asm volatile("" ::: "memory");
}
DI void cnt_wait(const lds_char* l16, int k, unsigned n) { cnt_wait(l16, k, n, cnt_peek(l16)); }

typedef __attribute__((address_space(1))) unsigned gu32;
constexpr int SCR_OFF = 65536;
constexpr int XFS = 1552;
constexpr int XREG = 6304;
constexpr int OSTG = 4096;
constexpr int LDS_ID = 256;
constexpr int LDS_W0 = LDS_ID + 2048;
constexpr int LDS_X = LDS_W0 + 2 * WMAT_BYTES;
constexpr int LDS_W2 = LDS_X + 8 * XREG;
constexpr int LDS_O = LDS_W2 + 2 * WMAT_BYTES;
constexpr int LDS_RAW = LDS_W2;
constexpr int LDS_RAW_END = LDS_RAW + 73728;
constexpr int LDS_TOTAL = LDS_RAW_END;
static_assert(LDS_O + 8 * OSTG <= LDS_RAW_END && LDS_O + 2 * WMAT_BYTES <= LDS_RAW_END, "staging");
static_assert(LDS_TOTAL <= 163840, "lds");

DI void dma_tile(const char* row, unsigned voff, unsigned lds) {
  const char* r1 = row + FRAMEB; const char* r2 = row + 2 * (size_t)FRAMEB; const char* r3 = row + 3 * (size_t)FRAMEB;
  unsigned keep;
  asm volatile(
      "s_mov_b32 %0, m0\n\t"
      "s_mov_b32 m0, %6\n\ts_nop 0\n\t"
      "global_load_lds_dwordx4 %1, %2 nt\n\t"
      "s_add_u32 m0, m0, %7\n\ts_nop 0\n\t"
      "global_load_lds_dwordx4 %1, %3 nt\n\t"
      "s_add_u32 m0, m0, %7\n\ts_nop 0\n\t"
      "global_load_lds_dwordx4 %1, %4 nt\n\t"
      "s_add_u32 m0, m0, %7\n\ts_nop 0\n\t"
      "global_load_lds_dwordx4 %1, %5 nt\n\t"
      "s_mov_b32 exec_hi, 0\n\t"
      "global_load_lds_dwordx4 %1, %5 offset:1024 nt\n\t"
      "s_sub_u32 m0, m0, %7\n\ts_nop 0\n\t"
      "global_load_lds_dwordx4 %1, %4 offset:1024 nt\n\t"
      "s_sub_u32 m0, m0, %7\n\ts_nop 0\n\t"
      "global_load_lds_dwordx4 %1, %3 offset:1024 nt\n\t"
      "s_sub_u32 m0, m0, %7\n\ts_nop 0\n\t"
      "global_load_lds_dwordx4 %1, %2 offset:1024 nt\n\t"
      "s_mov_b32 exec_hi, -1\n\t"
      "s_mov_b32 m0, %0"
      : "=&s"(keep) : "v"(voff), "s"(row), "s"(r1), "s"(r2), "s"(r3), "s"(lds), "i"(XFS) : "memory", "scc");
}
DI void dma_tile_sc1(const char* row, unsigned voff, unsigned lds) {
  const char* r1 = row + FRAMEB; const char* r2 = row + 2 * (size_t)FRAMEB; const char* r3 = row + 3 * (size_t)FRAMEB;
  unsigned keep;
  asm volatile(
      "s_mov_b32 %0, m0\n\t"
      "s_mov_b32 m0, %6\n\ts_nop 0\n\t"
      "global_load_lds_dwordx4 %1, %2 sc1\n\t"
      "s_add_u32 m0, m0, %7\n\ts_nop 0\n\t"
      "global_load_lds_dwordx4 %1, %3 sc1\n\t"
      "s_add_u32 m0, m0, %7\n\ts_nop 0\n\t"
      "global_load_lds_dwordx4 %1, %4 sc1\n\t"
      "s_add_u32 m0, m0, %7\n\ts_nop 0\n\t"
      "global_load_lds_dwordx4 %1, %5 sc1\n\t"
      "s_mov_b32 exec_hi, 0\n\t"
      "global_load_lds_dwordx4 %1, %5 offset:1024 sc1\n\t"
      "s_sub_u32 m0, m0, %7\n\ts_nop 0\n\t"
      "global_load_lds_dwordx4 %1, %4 offset:1024 sc1\n\t"
      "s_sub_u32 m0, m0, %7\n\ts_nop 0\n\t"
      "global_load_lds_dwordx4 %1, %3 offset:1024 sc1\n\t"
      "s_sub_u32 m0, m0, %7\n\ts_nop 0\n\t"
      "global_load_lds_dwordx4 %1, %2 offset:1024 sc1\n\t"
      "s_mov_b32 exec_hi, -1\n\t"
      "s_mov_b32 m0, %0"
      : "=&s"(keep) : "v"(voff), "s"(row), "s"(r1), "s"(r2), "s"(r3), "s"(lds), "i"(XFS) : "memory", "scc");
}
DI void dma_piece(const char* base, unsigned voff, unsigned lds) {
  unsigned keep;
  asm volatile("s_mov_b32 %0, m0\n\ts_mov_b32 m0, %3\n\ts_nop 0\n\tglobal_load_lds_dwordx4 %1, %2\n\ts_mov_b32 m0, %0"
               : "=&s"(keep) : "v"(voff), "s"(base), "s"(lds) : "memory");
}
#define VMWAIT(n) asm volatile("s_waitcnt vmcnt(" #n ")" ::: "memory")
#define LGWAIT() asm volatile("s_waitcnt lgkmcnt(0)" ::: "memory")

template <int CPR, int SS>
DI void raw_piece(const char* src, int piece, int lane, unsigned lds_img) {
  const unsigned q = (unsigned)(piece * 64 + lane);
  const unsigned row = (q * (unsigned)(((1 << 20) + CPR - 1) / CPR)) >> 20;
  const unsigned off = (SS == CPR * 16) ? 16u * (q ^ ((row >> 2) & 3u)) : row * (unsigned)(SS - CPR * 16) + 16u * (q ^ ((row >> 2) & 3u));
  dma_piece(src, off, lds_img + piece * 1024);
}
typedef short s16x4 __attribute__((ext_vector_type(4)));
DI void fold_read_rows(u32x4* d, const lds_char* img, int rb, int row0, int sw, int hh) {
  const int x = (sw >> 2) & 3;
  const lds_char* b0 = img + (row0 + sw) * rb + 16 * (hh ^ x);
  const lds_char* b1 = img + (row0 + sw) * rb + 16 * ((2 + hh) ^ x);
#pragma unroll
  for (int s = 0; s < 6; ++s) d[s] = lds_ld(((s & 1) ? b1 : b0) + 64 * (s >> 1));
}
DI void fold_read_cols(u32x4* d, const lds_char* img, int T, int lane) {
  const int hh = lane >> 5, cb = (lane >> 4) & 1, qq = (lane >> 2) & 3, pp = lane & 3;
  const int cc = 4 * T + 2 * cb + (pp & 1);
  const lds_char* b0 = img + (8 * hh + qq) * 192 + 16 * (cc ^ ((2 * hh) & 3)) + 8 * (pp >> 1);
  const lds_char* b1 = img + (8 * hh + qq + 4) * 192 + 16 * (cc ^ ((2 * hh + 1) & 3)) + 8 * (pp >> 1);
#pragma unroll
  for (int s = 0; s < 6; ++s) {
    s16x4 lo = __builtin_amdgcn_ds_read_tr16_b64_v4i16((__attribute__((address_space(3))) s16x4*)(b0 + s * 16 * 192));
    s16x4 hi = __builtin_amdgcn_ds_read_tr16_b64_v4i16((__attribute__((address_space(3))) s16x4*)(b1 + s * 16 * 192));
    d[s] = __builtin_bit_cast(u32x4, __builtin_shufflevector(lo, hi, 0, 1, 2, 3, 4, 5, 6, 7));
  }
}
DI void fold_compute(const u32x4 (&a)[18], const u32x4 (&b)[6], float sc, u32x4 (&res)[6]) {
  const f32x16 zero = {};
  f32x16 acc[3];
#pragma unroll
  for (int s = 0; s < 6; ++s)
#pragma unroll
    for (int Tin = 0; Tin < 3; ++Tin) acc[Tin] = MFMA(a[Tin * 6 + s], b[s], s == 0 ? zero : acc[Tin]);
#pragma unroll
  for (int Tin = 0; Tin < 3; ++Tin) {
#pragma unroll
    for (int i = 0; i < 16; ++i) acc[Tin][i] *= sc;
    res[2 * Tin] = pack8(acc[Tin], 0);
    res[2 * Tin + 1] = pack8(acc[Tin], 1);
  }
}
DI void fold_store(lds_char* wdst, const u32x4 (&res)[6]) {
#pragma unroll
  for (int s = 0; s < 6; ++s) lds_st(wdst + s * 1024, res[s]);
}

struct Stats { float s0, s1, q0, q1, rs, nb; };
DI void stat_pkt(const u32x4 (&xf)[6], f32x16 (&xs)[3], Stats& st, int k) {
  const int s = k >> 2, i = k & 3, T = s >> 1, e = 8 * (s & 1) + 2 * i;
  const float a = lo16(xf[s][i]), b = hi16(xf[s][i]);
  xs[T][e] = a; xs[T][e + 1] = b;
  if (k == 0) { st.s0 = a; st.s1 = b; st.q0 = a * a; st.q1 = b * b; }
  else { st.s0 += a; st.s1 += b; st.q0 = __builtin_fmaf(a, a, st.q0); st.q1 = __builtin_fmaf(b, b, st.q1); }
  PIN(st.s0); PIN(st.s1); PIN(st.q0); PIN(st.q1);
}
DI void stat_fin(Stats& st) {
  float s = xhalf_sum(st.s0 + st.s1), q = xhalf_sum(st.q0 + st.q1);
  float m = s * (1.f / CH);
  float var = __builtin_fmaxf(__builtin_fmaf(-m, m, q * (1.f / CH)), 0.f);
  st.rs = __builtin_amdgcn_rsqf(var + LN_EPS);
  st.nb = -m * st.rs;
  PIN(st.rs); PIN(st.nb);
}
DI void nrm_pkt(const f32x16 (&xs)[3], u32x4 (&hf)[6], const Stats& st, int k) {
  const int s = k >> 2, i = k & 3, T = s >> 1, e = 8 * (s & 1) + 2 * i;
  unsigned v = pk2(__builtin_fmaf(xs[T][e], st.rs, st.nb), __builtin_fmaf(xs[T][e + 1], st.rs, st.nb));
  PIN(v);
  hf[s][i] = v;
}
DI void cvt4(u32x4& d, const f32x16& a, int s) { d = pack8(a, s); PIN(d); }

constexpr int RD = 4;
constexpr int NWSEQ = 144;
DI constexpr int woff(int n) {
  n %= NWSEQ;
  if (n < 72) {
    int k = n % 36;
    if (k < 12) return LDS_W0 + (k & 1) * WMAT_BYTES + (k >> 1) * 1024;
    if (k < 24) return LDS_W0 + (6 + (k - 12)) * 1024;
    return LDS_W0 + WMAT_BYTES + (6 + (k - 24)) * 1024;
  }
  if (n < 108) return LDS_W2 + ((n - 72) % 18) * 1024;
  return LDS_W2 + WMAT_BYTES + (((n - 108) / 12) * 6 + (n - 108) % 6) * 1024;
}
constexpr int WB_BASE = LDS_W2;
struct Ctx {
  const lds_char* wa;
  const lds_char* wb;
  bool ms0, ms1, mg[4];
};
DI u32x4 wload(const Ctx& c, int n) {
  const int o = woff(n);
  return o < LDS_W2 ? lds_ld(c.wa + o) : lds_ld(c.wb + (o - WB_BASE));
}
#define WUSE(n) ring[(n) % RD]
#define WNEXT(n) ring[(n) % RD] = wload(c, (n) + RD)

struct Slot { int ty, T, s; };
DI constexpr Slot tslot(int m) {
  if (m < 12) return {m & 1, 0, m >> 1};
  if (m < 15) return {0, 1, m - 12};
  if (m < 17) return {2, 0, m - 15};
  if (m < 20) return {0, 1, m - 14};
  if (m < 23) return {0, 2, m - 20};
  if (m < 25) return {2, 1, m - 23};
  if (m < 28) return {0, 2, m - 22};
  if (m < 31) return {1, 1, m - 28};
  if (m < 33) return {2, 2, m - 31};
  if (m < 36) return {1, 1, m - 30};
  if (m < 42) return {1, 2, m - 36};
  return {3, (m - 42) >> 1, (m - 42) & 1};
}
DI constexpr int twidx(int m) {
  return m < 15 ? m : (m < 17 ? 15 : (m < 23 ? m - 2 : (m < 25 ? 21 : (m < 31 ? m - 4 : (m < 33 ? 27 : (m < 42 ? m - 6 : 36))))));
}
DI constexpr int sched_cnt(int m, int first, int last, int total) {
  return m <= first ? 0 : (m >= last ? total : (m - first) * total / (last - first));
}

struct TState { f32x16 g[3], v[3], sa; u32x4 hf[6], gp[2], vp[3][2], pf[2]; float e[4]; unsigned pk0, pk1; };
template <int K0, int K1>
DI void stat_pkts(const u32x4 (&xf)[6], f32x16 (&xs)[3], Stats& st) {
  if constexpr (K0 < K1) { stat_pkt(xf, xs, st, K0); stat_pkts<K0 + 1, K1>(xf, xs, st); }
}
template <int N0, int m, int M1, int HS, bool PE, bool OE>
DI void temporal_slots(const Ctx& c, u32x4 (&ring)[RD], TState& t, f32x16 (&xs)[3], const Stats& st,
                       const u32x4 (&nx)[6], f32x16 (&nxs)[3], Stats& nst, u32x4 (&nhf)[6], const f32x16 (&pxs)[3], lds_char* xw) {
  const f32x16 zero = {};
  if constexpr (m < M1) {
    constexpr Slot sl = tslot(m);
    constexpr int n = N0 + twidx(m);
    if constexpr (sl.ty == 0) { t.g[sl.T] = MFMA(WUSE(n), t.hf[sl.s], sl.s == 0 ? zero : t.g[sl.T]); WNEXT(n); }
    else if constexpr (sl.ty == 1) { t.v[sl.T] = MFMA(t.hf[sl.s], WUSE(n), sl.s == 0 ? zero : t.v[sl.T]); WNEXT(n); }
    else if constexpr (sl.ty == 2) t.sa = MFMA(t.hf[2 * sl.T + sl.s], t.gp[sl.s], (sl.T == 0 && sl.s == 0) ? zero : t.sa);
    else xs[sl.T] = MFMA(t.vp[sl.T][sl.s], t.pf[sl.s], xs[sl.T]);
    SB();
    if constexpr (m < 10) { nrm_pkt(xs, t.hf, st, 4 + 2 * m); nrm_pkt(xs, t.hf, st, 5 + 2 * m); }
    if constexpr (PE && m >= 2 && m < 8) { u32x4 a; cvt4(a, pxs[(m - 2) >> 1], m & 1); lds_st(xw + (m - 2) * 1024, a); }
    if constexpr (m == 12) cvt4(t.gp[0], t.g[0], 0);
    if constexpr (m == 13) cvt4(t.gp[1], t.g[0], 1);
    if constexpr (m == 14) cvt4(t.vp[0][0], t.v[0], 0);
    if constexpr (m == 15) cvt4(t.vp[0][1], t.v[0], 1);
    if constexpr (m == 21) { cvt4(t.gp[0], t.g[1], 0); }
    if constexpr (m == 22) { cvt4(t.gp[1], t.g[1], 1); }
    if constexpr (m == 29) { cvt4(t.gp[0], t.g[2], 0); }
    if constexpr (m == 30) { cvt4(t.gp[1], t.g[2], 1); }
    if constexpr (m == 35 || m == 36) {
#pragma unroll
      for (int k = 2 * (m - 35); k < 2 * (m - 35) + 2; ++k) {
        float s1 = t.sa[4 + k], s3 = t.sa[12 + k];
        asm("" : "+v"(s1)); asm("" : "+v"(s3));
        float a = c.ms0 ? s1 : t.sa[k];
        float b = c.ms0 ? s3 : t.sa[8 + k];
        t.e[k] = c.ms1 ? b : a; PIN(t.e[k]);
      }
    }
    if constexpr (m == 37) {
      float mx = max3(max3(t.e[0], t.e[1], t.e[2]), t.e[3], t.e[3]);
#pragma unroll
      for (int k = 0; k < 4; ++k) { t.e[k] = __builtin_amdgcn_exp2f(t.e[k] - mx); PIN(t.e[k]); }
    }
    if constexpr (m == 38) {
      float rinv = __builtin_amdgcn_rcpf((t.e[0] + t.e[1]) + (t.e[2] + t.e[3]));
      t.pk0 = pk2(t.e[0] * rinv, t.e[1] * rinv); t.pk1 = pk2(t.e[2] * rinv, t.e[3] * rinv);
      PIN(t.pk0); PIN(t.pk1);
    }
    if constexpr (m == 39) {
#pragma unroll
      for (int g = 0; g < 4; ++g) {
        t.pf[g >> 1][(g & 1) * 2 + 0] = c.mg[g] ? t.pk0 : 0u;
        t.pf[g >> 1][(g & 1) * 2 + 1] = c.mg[g] ? t.pk1 : 0u;
      }
      PIN(t.pf[0]); PIN(t.pf[1]);
    }
    if constexpr (m == 40) { cvt4(t.vp[1][0], t.v[1], 0); cvt4(t.vp[1][1], t.v[1], 1); }
    if constexpr (m == 44) { cvt4(t.vp[2][0], t.v[2], 0); cvt4(t.vp[2][1], t.v[2], 1); }
    if constexpr (m < 35) stat_pkts<sched_cnt(m, HS + 2, 35, 20), sched_cnt(m + 1, HS + 2, 35, 20)>(nx, nxs, nst);
    if constexpr (m == 41) stat_pkts<20, 22>(nx, nxs, nst);
    if constexpr (m == 42) stat_pkts<22, 24>(nx, nxs, nst);
    if constexpr (m == 43) stat_fin(nst);
    if constexpr (m == 45) { nrm_pkt(nxs, nhf, nst, 0); nrm_pkt(nxs, nhf, nst, 1); }
    if constexpr (m == 46) { nrm_pkt(nxs, nhf, nst, 2); nrm_pkt(nxs, nhf, nst, 3); }
    if constexpr (OE && m == 46) { u32x4 a; cvt4(a, xs[0], 0); lds_st(xw, a); }
    if constexpr (OE && m == 47) { u32x4 a; cvt4(a, xs[0], 1); lds_st(xw + 1024, a); }
    SB();
    temporal_slots<N0, m + 1, M1, HS, PE, OE>(c, ring, t, xs, st, nx, nxs, nst, nhf, pxs, xw);
  }
}

struct SMState { float m0, m1, mx, l0, l1, rinv; };
template <int u>
DI void sm_unit(f32x16& a, f32x16& b, SMState& s, u32x4 (&pf)[4]) {
  if constexpr (u < 4) {
    f32x16& x = (u < 2) ? a : b;
    const int o = 8 * (u & 1);
    if (u == 0) { s.m0 = max3(x[0], x[1], x[2]); s.m1 = max3(x[3], x[4], x[5]); s.m0 = max3(s.m0, x[6], x[7]); }
    else { s.m0 = max3(s.m0, x[o], x[o + 1]); s.m1 = max3(s.m1, x[o + 2], x[o + 3]); s.m0 = max3(s.m0, x[o + 4], x[o + 5]); s.m1 = max3(s.m1, x[o + 6], x[o + 7]); }
    PIN(s.m0); PIN(s.m1);
  } else if constexpr (u == 4) {
    s.mx = xhalf_max(__builtin_fmaxf(s.m0, s.m1));
    if (__builtin_expect(__builtin_amdgcn_ballot_w64(!(__builtin_fabsf(s.mx) <= 40.f)) != 0ull, 0)) {
#pragma unroll
      for (int i = 0; i < 16; ++i) { a[i] -= s.mx; b[i] -= s.mx; }
    }
  } else if constexpr (u < 21) {
    constexpr int k = u - 5;
    f32x16& x = (k < 8) ? a : b;
    constexpr int o = 2 * (k & 7);
    float e0 = __builtin_amdgcn_exp2f(x[o]), e1 = __builtin_amdgcn_exp2f(x[o + 1]);
    PIN(e0); PIN(e1);
    x[o] = e0; x[o + 1] = e1;
    if constexpr (k >= 2) {
      f32x16& y = (k - 1 < 8) ? a : b;
      constexpr int o1 = 2 * ((k - 1) & 7);
      if constexpr (k == 2) { s.l0 = a[0] + y[o1]; s.l1 = a[1] + y[o1 + 1]; } else { s.l0 += y[o1]; s.l1 += y[o1 + 1]; }
      PIN(s.l0); PIN(s.l1);
    }
  } else if constexpr (u == 21) {
    s.rinv = __builtin_amdgcn_rcpf(xhalf_sum((s.l0 + b[14]) + (s.l1 + b[15]))); PIN(s.rinv);
  } else {
    const int k = u - 22;
    f32x16& x = (k < 4) ? a : b;
    const int o = 4 * (k & 3);
    unsigned p0 = pk2(x[o] * s.rinv, x[o + 1] * s.rinv), p1 = pk2(x[o + 2] * s.rinv, x[o + 3] * s.rinv);
    PIN(p0); PIN(p1);
    pf[k >> 1][2 * (k & 1)] = p0; pf[k >> 1][2 * (k & 1) + 1] = p1;
  }
}

__global__ __launch_bounds__(512, 2) NOPK void swin_kernel(const char* __restrict__ x, const char* __restrict__ wqkv_t,
                                                           const char* __restrict__ wo_t, const char* __restrict__ wqkv_s,
                                                           const char* __restrict__ wo_s, char* __restrict__ out,
                                                           char* __restrict__ ws) {
  extern __shared__ __attribute__((aligned(16))) char smem_[];
  lds_char* smem = (lds_char*)smem_;
  const unsigned smem_a = (unsigned)(size_t)smem;
  const int tid = threadIdx.x, lane = tid & 63;
  const int wid8 = __builtin_amdgcn_readfirstlane(tid >> 6);
  const int grp = wid8 >> 2, wid = wid8 & 3;
  const int r = lane & 31, hh = lane >> 5;
  const int p = r >> 2, f = r & 3;
  const int sw = (r & 0x13) | ((r & 4) << 1) | ((r & 8) >> 1);
  const unsigned dvoff = lane * 16;
  const unsigned xr_a = smem_a + LDS_X + wid8 * XREG;

  constexpr int FULL = 0, SONLY = 2, NONE = 3;
  const int bid = blockIdx.x;
  const int xj = bid >> 3, ub = (bid & 7) * 98;
  const bool shelp = xj == 8 || xj == 9, thelp = xj < 8 && grp == 0;
  const int li = shelp ? 2 * (xj - 8) + grp : (xj >> 1) & 3, ti = xj & 1;
  const int lunit = ub + 94 + li, hslot = ((bid & 7) * 4 + li) * 4 + wid;
  const int jj = xj < 8 ? xj : xj - 2;
  int unit = shelp ? ub + 90 + li : ub + jj + 30 * grp;
  int nmode = shelp ? SONLY : (grp == 0 ? FULL : NONE), nunit = shelp ? lunit : ub + jj + 60;
  const lds_char* l16 = smem + lane * 16;
  constexpr int C_TW = 5, C_MS = 6, C_RAW = 7, C_RDT = 8;
  constexpr int IMG = WMAT_BYTES;
  gu32* hfl = (gu32*)(ws + hslot * 256);
  unsigned hev, cpf = 0, hepoch;
  {
    {
      unsigned long long pc; asm volatile("s_getpc_b64 %0" : "=s"(pc));
      const char* cb = (const char*)(pc & ~255ull);
      const unsigned o = (unsigned)(((xj & 7) * 8 + wid8) * (CODEPF / 64) + (lane * 64 < CODEPF / 64 ? lane * 64 : 0));
      asm volatile("global_load_dword %0, %1, %2" : "+v"(cpf) : "v"(o), "s"(cb) : "memory");
    }
    asm volatile("global_load_dword %0, %1, %2 sc1" : "=v"(hev) : "v"(0u), "s"(hfl) : "memory");
    {
      int pc = (wid8 * 7 + xj * 5) % 54;
#pragma nounroll
      for (int i = 0; i < 7; ++i) { raw_piece<36, 576>(wqkv_t, pc, lane, smem_a + LDS_RAW); pc = pc == 53 ? 0 : pc + 1; }
      pc = (wid8 * 3 + xj) % 18;
#pragma nounroll
      for (int i = 0; i < 3; ++i) { raw_piece<12, 192>(wo_t, pc, lane, smem_a + LDS_RAW + 3 * IMG); pc = pc == 17 ? 0 : pc + 1; }
    }
    if (tid < 64) ((lds_u32*)(smem + LDS_CNT))[tid] = 0u;
    const int u0 = thelp ? lunit : unit, wy0 = u0 / NWIN, wx0 = u0 % NWIN;
    const char* x0 = x + (size_t)(wy0 * 8 + 2 * wid + (thelp ? ti : 0)) * IMGROWB + (size_t)(wx0 * 8) * ROWB;
    asm volatile("s_waitcnt vmcnt(0) lgkmcnt(0)\n\ts_barrier" : "+v"(hev), "+v"(cpf) :: "memory");
    hepoch = __builtin_amdgcn_readfirstlane(hev);
    if (grp == 0) dma_tile(x0, dvoff, xr_a);
    u32x4 fa[18], fb[6], res[6];
    auto job_read = [&](int j) __attribute__((always_inline)) NOPK {
      const lds_char* aimg = smem + LDS_RAW + (j < 3 ? 384 : 0);
#pragma unroll
      for (int Tin = 0; Tin < 3; ++Tin) fold_read_rows(fa + 6 * Tin, aimg, 576, 32 * Tin, sw, hh);
      if (j < 3) fold_read_cols(fb, smem + LDS_RAW + 3 * IMG, j, lane);
      else fold_read_rows(fb, smem + LDS_RAW + 192, 576, 32 * (j - 3), sw, hh);
    };
    if (grp == 1) {
      __builtin_amdgcn_s_setprio(1);
      const int ja = wid < 2 ? wid : 2 * wid - 2;
#pragma nounroll
      for (int stage = 0; stage < 2; ++stage) {
        lds_char* wdst = smem + (stage ? LDS_W2 : LDS_W0) + lane * 16;
        u32x4 res2[6];
        job_read(ja);
        fold_compute(fa, fb, ja < 3 ? 1.f : QK_SCALE, res);
        if (stage == 0) fold_store(wdst + (ja < 3 ? IMG : 0) + (ja % 3) * 6144, res);
        if (wid >= 2) {
          job_read(ja + 1);
          cnt_arrive(l16, dvoff, C_RDT);
          fold_compute(fa, fb, QK_SCALE, res2);
        } else cnt_arrive(l16, dvoff, C_RDT);
        if (stage == 0) {
          if (wid >= 2) fold_store(wdst + ((ja + 1) % 3) * 6144, res2);
          cnt_arrive(l16, dvoff, C_TW);
          cnt_wait(l16, C_RDT, 4);
          int pc = (wid * 14 + xj * 5) % 54;
#pragma nounroll
          for (int i = 0; i < 14; ++i) { raw_piece<36, 576>(wqkv_s, pc, lane, smem_a + LDS_RAW); pc = pc == 53 ? 0 : pc + 1; }
          pc = (wid * 5 + xj) % 18;
#pragma nounroll
          for (int i = 0; i < 5; ++i) { raw_piece<12, 192>(wo_s, pc, lane, smem_a + LDS_RAW + 3 * IMG); pc = pc == 17 ? 0 : pc + 1; }
          dma_tile(x0, dvoff, xr_a);
          VMWAIT(8);
          cnt_arrive(l16, dvoff, C_RAW);
          cnt_wait(l16, C_RAW, 4);
        } else {
          cnt_wait(l16, C_RDT, 8);
          fold_store(wdst + (ja < 3 ? IMG : 0) + (ja % 3) * 6144, res);
          if (wid >= 2) fold_store(wdst + ((ja + 1) % 3) * 6144, res2);
          cnt_arrive(l16, dvoff, C_MS);
        }
      }
      __builtin_amdgcn_s_setprio(0);
    } else if (wid8 == 3) {
      u32x4 idf[2];
#pragma unroll
      for (int s = 0; s < 2; ++s)
#pragma unroll
        for (int d = 0; d < 4; ++d)
          idf[s][d] = (s == (sw >> 4) && hh == ((sw >> 3) & 1) && d == ((sw & 7) >> 1)) ? (0x3F80u << (16 * (sw & 1))) : 0u;
      lds_st(smem + LDS_ID + lane * 16, idf[0]); lds_st(smem + LDS_ID + 1024 + lane * 16, idf[1]);
      cnt_arrive(l16, dvoff, C_MS);
    }
  }

  const int cW = 1 + 2 * grp, cR = 2 + 2 * grp;
  auto laddr = [&](unsigned off) __attribute__((always_inline)) NOPK { off += smem_a; asm("" : "+v"(off)); return (lds_char*)(size_t)off; };
  const unsigned xg = LDS_X + grp * 4 * XREG;
  lds_char* xw = laddr(xg + f * XREG + (hh * 32 + 8 * wid + p) * 16);
  const lds_char* xr = laddr(xg + wid * XREG + lane * 16);
  const lds_char* xl = laddr(xg + wid * XREG + f * XFS + p * ROWB + hh * 16);
  lds_char* ow0 = laddr(LDS_O + wid8 * OSTG + r * 64 + 16 * (hh ^ ((r >> 1) & 3)));
  lds_char* ow1 = laddr(LDS_O + wid8 * OSTG + r * 64 + 16 * ((2 + hh) ^ ((r >> 1) & 3)));
  const lds_char* ord = laddr(LDS_O + wid8 * OSTG + lane * 16);
  const f32x16 zero = {};
  Ctx c;
  c.wa = l16; c.wb = laddr(WB_BASE + lane * 16);
  {
    c.ms0 = (p >> 1) & 1;
    c.ms1 = (p >> 2) & 1;
    const bool valid = (hh == (p & 1));
#pragma unroll
    for (int g = 0; g < 4; ++g) c.mg[g] = valid && (p >> 1) == g;
  }
  unsigned ooff = (lane >> 5) * 2 * IMGROWB + ((lane >> 2) & 7) * ROWB + 16 * ((lane & 3) ^ ((lane >> 3) & 3));
  asm("" : "+v"(ooff));
  auto orsrc = __builtin_amdgcn_make_buffer_rsrc((void*)out, 0, 4 * FRAMEB, 0x00020000);

  f32x16 xsA[3], xsB[3], xsC[3]; Stats stA, stB, stC; u32x4 hfA[6];
  u32x4 x1[2][6], h2[2][6];
  u32x4 ring[RD];
  bool first = true, cold = true;
  unsigned ph = 0, pk;
  const char* nxu; int nstep; bool more;

  auto cold_start2 = [&]() __attribute__((always_inline)) NOPK {
    const int wy = unit / NWIN, wx = unit % NWIN;
    u32x4 xin[6];
#pragma unroll
    for (int s = 0; s < 6; ++s) xin[s] = lds_ld(xl + 32 * s);
    LGWAIT();
    dma_tile(x + (size_t)(wy * 8 + 2 * wid + (thelp ? 0 : 1)) * IMGROWB + (size_t)(wx * 8) * ROWB, dvoff, xr_a);
    stat_pkts<0, 24>(xin, xsA, stA);
    stat_fin(stA);
#pragma unroll
    for (int k = 0; k < 4; ++k) nrm_pkt(xsA, hfA, stA, k);
    cnt_wait(l16, C_TW, 4);
#pragma unroll
    for (int n = 0; n < RD; ++n) ring[n] = wload(c, n);
  };
  auto next_setup = [&]() __attribute__((always_inline)) NOPK {
    more = nmode != NONE;
    if (nmode == SONLY) { nxu = ws + SCR_OFF + (size_t)(hslot * 2) * 1536; nstep = 1536; }
    else { const int ny = nunit / NWIN, nx = nunit % NWIN; nxu = x + (size_t)(ny * 8 + 2 * wid) * IMGROWB + (size_t)(nx * 8) * ROWB; nstep = IMGROWB; }
  };
  auto phaseT = [&]() __attribute__((always_inline)) NOPK {
    TState t2;
    {
      TState t; u32x4 xin[6];
      t.hf[0] = hfA[0];
      temporal_slots<0, 0, 11, 10, false, false>(c, ring, t, xsA, stA, xin, xsB, stB, t2.hf, xsA, xw);
      if (cold) { VMWAIT(0); } else { VMWAIT(10); }
      if (cold && thelp && lane == 0)
        __hip_atomic_store(hfl + 32 + 16 * ti, hepoch + 1u, __ATOMIC_RELAXED, __HIP_MEMORY_SCOPE_AGENT);
#pragma unroll
      for (int s = 0; s < 6; ++s) xin[s] = lds_ld(xl + 32 * s);
      cnt_arrive(l16, dvoff, cR);
      temporal_slots<0, 11, 46, 10, false, false>(c, ring, t, xsA, stA, xin, xsB, stB, t2.hf, xsA, xw);
      pk = cnt_peek(l16);
      temporal_slots<0, 46, 48, 10, false, false>(c, ring, t, xsA, stA, xin, xsB, stB, t2.hf, xsA, xw);
    }
    {
      temporal_slots<36, 0, 2, 19, true, true>(c, ring, t2, xsB, stB, x1[0], xsC, stC, h2[0], xsA, xw);
      cnt_wait(l16, cR, ph + 4, pk);
      temporal_slots<36, 2, 8, 19, true, true>(c, ring, t2, xsB, stB, x1[0], xsC, stC, h2[0], xsA, xw);
      cnt_arrive(l16, dvoff, cW);
      temporal_slots<36, 8, 17, 19, true, true>(c, ring, t2, xsB, stB, x1[0], xsC, stC, h2[0], xsA, xw);
      pk = cnt_peek(l16);
      temporal_slots<36, 17, 20, 19, true, true>(c, ring, t2, xsB, stB, x1[0], xsC, stC, h2[0], xsA, xw);
      cnt_wait(l16, cW, ph + 4, pk);
#pragma unroll
      for (int s = 0; s < 6; ++s) x1[0][s] = lds_ld(xr + s * 1024);
      cnt_arrive(l16, dvoff, cR);
      if (first) cnt_wait(l16, C_MS, 5);
      temporal_slots<36, 20, 43, 19, true, true>(c, ring, t2, xsB, stB, x1[0], xsC, stC, h2[0], xsA, xw);
      pk = cnt_peek(l16);
      temporal_slots<36, 43, 46, 19, true, true>(c, ring, t2, xsB, stB, x1[0], xsC, stC, h2[0], xsA, xw);
      cnt_wait(l16, cR, ph + 8, pk);
      temporal_slots<36, 46, 48, 19, true, true>(c, ring, t2, xsB, stB, x1[0], xsC, stC, h2[0], xsA, xw);
    }
  };
  auto phaseS = [&](auto SOC) __attribute__((always_inline)) NOPK {
    constexpr bool SO = SOC.v != 0;
    const int wy = unit / NWIN, wx = unit % NWIN;
    f32x16 sa[2][2]; u32x4 gq[6];
    {
      f32x16 g[3], xsD[3]; Stats stD;
      sfor<0, 10>(LAM(M) {
        constexpr int m = M.v, n = 72 + m, T = m / 6, s = m % 6;
        g[T] = MFMA(WUSE(n), h2[0][s], s == 0 ? zero : g[T]); WNEXT(n);
        SB();
        if constexpr (m < 2) {
          if constexpr (!SO) {
            u32x4 a, b; cvt4(a, xsB[1 + m], 0); lds_st(xw + (2 + 2 * m) * 1024, a); cvt4(b, xsB[1 + m], 1); lds_st(xw + (3 + 2 * m) * 1024, b);
            if constexpr (m == 1) cnt_arrive(l16, dvoff, cW);
          }
        }
        if constexpr (m < 5) {
#pragma unroll
          for (int k = 4 + 4 * m; k < 8 + 4 * m; ++k) nrm_pkt(xsC, h2[0], stC, k);
        }
        if constexpr (m == 7) { cvt4(gq[0], g[0], 0); cvt4(gq[1], g[0], 1); }
        if constexpr (!SO && m == 8) pk = cnt_peek(l16);
        SB();
      });
      if constexpr (!SO) {
        cnt_wait(l16, cW, ph + 8, pk);
#pragma unroll
        for (int s = 0; s < 6; ++s) x1[1][s] = lds_ld(xr + s * 1024);
      } else {
        VMWAIT(10);
#pragma unroll
        for (int s = 0; s < 6; ++s) x1[1][s] = lds_ld(xl + 32 * s);
      }
      sfor<10, 24>(LAM(M) {
        constexpr int m = M.v;
        if constexpr (m < 12) { constexpr int n = 72 + m; g[1] = MFMA(WUSE(n), h2[0][m - 6], g[1]); WNEXT(n); }
        else if constexpr (m < 14) sa[0][0] = MFMA(h2[0][m - 12], gq[m - 12], m == 12 ? zero : sa[0][0]);
        else if constexpr (m < 20) { constexpr int n = 72 + m - 2; g[2] = MFMA(WUSE(n), h2[0][m - 14], m == 14 ? zero : g[2]); WNEXT(n); }
        else sa[0][0] = MFMA(h2[0][m - 18], gq[m - 18], sa[0][0]);
        SB();
        if constexpr (m == 13) {
          LGWAIT();
          if (!SO && nmode == FULL) dma_tile(nxu, dvoff, xr_a);
        }
        if constexpr (m == 14) { cvt4(gq[2], g[1], 0); cvt4(gq[3], g[1], 1); }
        if constexpr (m == 21) { cvt4(gq[4], g[2], 0); cvt4(gq[5], g[2], 1); }
        stat_pkts<sched_cnt(m, 11, 20, 24), sched_cnt(m + 1, 11, 20, 24)>(x1[1], xsD, stD);
        if constexpr (m == 19) stat_fin(stD);
        if constexpr (m >= 20) {
#pragma unroll
          for (int k = 6 * (m - 20); k < 6 * (m - 19); ++k) nrm_pkt(xsD, h2[1], stD, k);
        }
        SB();
      });
    }
    u32x4 pf[2][4];
    {
      f32x16 g[3]; u32x4 gp[2][2]; SMState sm;
      sfor<0, 36>(LAM(M) {
        constexpr int m = M.v;
        if constexpr (m < 6) sa[0][1] = MFMA(h2[1][m], gq[m], m == 0 ? zero : sa[0][1]);
        else if constexpr (m < 18) { constexpr int n = 90 + m - 6, T = (m - 6) / 6, s = (m - 6) % 6; g[T] = MFMA(WUSE(n), h2[1][s], s == 0 ? zero : g[T]); WNEXT(n); }
        else if constexpr (m < 22) { constexpr int kt = (m - 18) & 1, s = (m - 18) >> 1; sa[1][kt] = MFMA(h2[kt][s], gp[0][s], s == 0 ? zero : sa[1][kt]); }
        else if constexpr (m < 28) { constexpr int n = 90 + m - 10, s = m - 22; g[2] = MFMA(WUSE(n), h2[1][s], s == 0 ? zero : g[2]); WNEXT(n); }
        else { constexpr int T = (m - 28) / 4 + 1, kt = (m - 28) & 1, s = ((m - 28) >> 1) & 1; sa[1][kt] = MFMA(h2[kt][2 * T + s], gp[T & 1][s], sa[1][kt]); }
        SB();
        if constexpr (m == 14) { cvt4(gp[0][0], g[0], 0); cvt4(gp[0][1], g[0], 1); }
        if constexpr (m == 20) { cvt4(gp[1][0], g[1], 0); cvt4(gp[1][1], g[1], 1); }
        if constexpr (m == 30) { cvt4(gp[0][0], g[2], 0); cvt4(gp[0][1], g[2], 1); }
        if constexpr (m == 2 || m == 3) sm_unit<m - 2>(sa[0][0], sa[0][1], sm, pf[0]);
        if constexpr (m >= 8) sm_unit<m - 6>(sa[0][0], sa[0][1], sm, pf[0]);
        SB();
      });
    }
    if (!SO && nmode == SONLY) {
      for (int spin = 0; spin < (1 << 22); ++spin) {
        const unsigned f0 = __hip_atomic_load(hfl + 32, __ATOMIC_RELAXED, __HIP_MEMORY_SCOPE_AGENT);
        const unsigned f1 = __hip_atomic_load(hfl + 48, __ATOMIC_RELAXED, __HIP_MEMORY_SCOPE_AGENT);
        if (__builtin_amdgcn_readfirstlane(((f0 - hepoch) ^ 1u) | ((f1 - hepoch) ^ 1u)) == 0u) break;
        __builtin_amdgcn_s_sleep(8);
      }
      __builtin_amdgcn_fence(__ATOMIC_ACQUIRE, "agent");
      asm volatile("" ::: "memory");
      if (lane == 0) __hip_atomic_store(hfl, hepoch + 1u, __ATOMIC_RELAXED, __HIP_MEMORY_SCOPE_AGENT);
      dma_tile_sc1(nxu, dvoff, xr_a);
    }
    {
      u32x4 vp[3][4], idf[2], xin[6], ov[2];
      f32x16 acc[12]; SMState sm;
      const unsigned obase = wid * FRAMEB + (wy * 8) * IMGROWB + (wx * 8) * ROWB;
      sfor<0, 81>(LAM(M) {
        constexpr int m = M.v, ch = m / 6, k = m % 6;
        constexpr int CT[12] = {0, 0, 0, 1, 0, 0, 1, 0, 1, 1, 1, 1};
        constexpr int CA[12] = {0, 1, 2, 0, 3, 4, 1, 5, 3, 2, 4, 5};
        if constexpr (m == 48) {
          if (!SO && more) {
            VMWAIT(2);
#pragma unroll
            for (int s = 0; s < 6; ++s) xin[s] = lds_ld(xl + 32 * s);
            LGWAIT();
            if (nmode == SONLY) dma_tile_sc1(nxu + nstep, dvoff, xr_a); else dma_tile(nxu + nstep, dvoff, xr_a);
          } else {
#pragma unroll
            for (int s = 0; s < 6; ++s) xin[s] = u32x4{};
          }
        }
        if constexpr (ch < 12) {
          if constexpr (CT[ch] == 0) {
            constexpr int vc = CA[ch], n = 108 + 6 * vc + k;
            acc[ch] = MFMA(h2[vc & 1][k], WUSE(n), k == 0 ? zero : acc[ch]); WNEXT(n);
          } else {
            constexpr int q = CA[ch] / 3, T = CA[ch] % 3;
            if constexpr (k < 2) acc[ch] = MFMA(idf[k], x1[q][2 * T + k], k == 0 ? zero : acc[ch]);
            else acc[ch] = MFMA(vp[T][k - 2], pf[q][k - 2], acc[ch]);
          }
          SB();
        }
        if constexpr (m == 12) { idf[0] = lds_ld(c.wa + LDS_ID); idf[1] = lds_ld(c.wa + LDS_ID + 1024); }
        if constexpr (m >= 2 && m < 42) {
          sfor<sched_cnt(m, 2, 42, 30), sched_cnt(m + 1, 2, 42, 30)>(LAM(U) { sm_unit<U.v>(sa[1][0], sa[1][1], sm, pf[1]); });
        }
        if constexpr (ch >= 1 && ch <= 12) {
          constexpr int pc = ch - 1;
          if constexpr (CT[pc] == 0) {
            if constexpr (k == 2 || k == 3) cvt4(vp[CA[pc] >> 1][2 * (CA[pc] & 1) + k - 2], acc[pc], k - 2);
          } else {
            constexpr int ob = (pc & 1) * 2048;
            if constexpr (k == 2) { u32x4 a; cvt4(a, acc[pc], 0); lds_st(ow0 + ob, a); }
            if constexpr (k == 3) { u32x4 a; cvt4(a, acc[pc], 1); lds_st(ow1 + ob, a); asm volatile("" ::: "memory"); }
            if constexpr (k == 4) { ov[0] = lds_ld(ord + ob); ov[1] = lds_ld(ord + ob + 1024); asm volatile("" ::: "memory"); }
          }
        }
        if constexpr (ch >= 2 && k == 1) {
          constexpr int pc = ch - 2;
          if constexpr (CT[pc] == 1) {
            constexpr int q = CA[pc] / 3, T = CA[pc] % 3;
            SB();
            __builtin_amdgcn_raw_buffer_store_b128(ov[0], orsrc, ooff, obase + q * IMGROWB + 64 * T, STORE_AUX);
            __builtin_amdgcn_raw_buffer_store_b128(ov[1], orsrc, ooff, obase + (4 + q) * IMGROWB + 64 * T, STORE_AUX);
            SB(); asm volatile("s_nop 1" ::: "memory"); SB();
          }
        }
        if constexpr (!SO && m >= 49) {
          stat_pkts<sched_cnt(m, 49, 70, 24), sched_cnt(m + 1, 49, 70, 24)>(xin, xsA, stA);
          if constexpr (m == 70) stat_fin(stA);
          if constexpr (m == 72 || m == 73) { nrm_pkt(xsA, hfA, stA, 2 * (m - 72)); nrm_pkt(xsA, hfA, stA, 2 * (m - 72) + 1); }
        }
        SB();
      });
    }
  };

  VMWAIT(0);
  if (shelp && grp == 1) __builtin_amdgcn_s_setprio(1);
  cold_start2();
  if (thelp) {
    TState t; u32x4 xin[6], hfB[6];
    t.hf[0] = hfA[0];
    temporal_slots<0, 0, 11, 10, false, false>(c, ring, t, xsA, stA, xin, xsB, stB, hfB, xsA, xw);
    VMWAIT(0);
#pragma unroll
    for (int s = 0; s < 6; ++s) xin[s] = lds_ld(xl + 32 * s);
    cnt_arrive(l16, dvoff, cR);
    temporal_slots<0, 11, 48, 10, false, false>(c, ring, t, xsA, stA, xin, xsB, stB, hfB, xsA, xw);
    cnt_wait(l16, cR, 4);
#pragma unroll
    for (int s = 0; s < 6; ++s) lds_st(xw + s * 1024, pack8(xsA[s >> 1], s & 1));
    cnt_arrive(l16, dvoff, cW);
#pragma unroll
    for (int n = 0; n < RD; ++n) ring[n] = wload(c, n);
    cnt_wait(l16, cW, 4);
#pragma unroll
    for (int s = 0; s < 6; ++s) xin[s] = lds_ld(xr + s * 1024);
#pragma unroll
    for (int s = 0; s < 6; ++s) lds_st((lds_char*)xl + 32 * s, xin[s]);
    asm volatile("" ::: "memory");
#pragma unroll
    for (int k = 0; k < 6; ++k) {
      const unsigned q = k * 64 + lane, row = (q * 683u) >> 16, n = q - 96u * row;
      xin[k] = lds_ld(smem + LDS_X + wid8 * XREG + row * XFS + 16 * n);
    }
    LGWAIT();
    {
      const int wy = unit / NWIN, wx = unit % NWIN;
      dma_tile(x + (size_t)(wy * 8 + 2 * wid + 1) * IMGROWB + (size_t)(wx * 8) * ROWB, dvoff, xr_a);
    }
    auto srs = __builtin_amdgcn_make_buffer_rsrc((void*)(ws + SCR_OFF), 0, 4 * FRAMEB, 0x00020000);
#pragma unroll
    for (int k = 0; k < 6; ++k) {
      const unsigned q = k * 64 + lane, row = (q * 683u) >> 16, n = q - 96u * row;
      __builtin_amdgcn_raw_buffer_store_b128(xin[k], srs, row * FRAMEB + 16 * n + (hslot * 2 + ti) * 1536, 0, 16);
    }
#pragma unroll
    for (int T = 0; T < 3; ++T) xsA[T] = xsB[T];
    stA = stB; hfA[0] = hfB[0];
    ph = 4;
  }
  for (;;) {
    next_setup();
    phaseT();
    phaseS(IC<0>{});
    if (nmode != FULL) break;
    ph += 8;
    unit = nunit; nmode = NONE;
    first = false; cold = false;
  }
  if (nmode == SONLY) {
    unit = nunit;
    __builtin_amdgcn_s_setprio(0);
#pragma unroll
    for (int T = 0; T < 3; ++T) xsC[T] = xsA[T];
    stC = stA; h2[0][0] = hfA[0];
#pragma unroll
    for (int s = 0; s < 6; ++s) x1[0][s] = pack8(xsA[s >> 1], s & 1);
#pragma unroll
    for (int n = 0; n < RD; ++n) ring[n] = wload(c, 72 + n);
    phaseS(IC<1>{});
  }
}

extern "C" void kernel_launch(void* const* d_in, const int* in_sizes, int n_in, void* d_out, int out_size,
                              void* d_ws, size_t ws_size, hipStream_t stream) {
  (void)hipFuncSetAttribute((const void*)swin_kernel, hipFuncAttributeMaxDynamicSharedMemorySize, LDS_TOTAL);
  swin_kernel<<<NWG, 512, LDS_TOTAL, stream>>>((const char*)d_in[0], (const char*)d_in[1], (const char*)d_in[2],
                                               (const char*)d_in[3], (const char*)d_in[4], (char*)d_out, (char*)d_ws);
}
```

```cpp
#include <hip/hip_runtime.h>
#include <hip/hip_bf16.h>
#include <stdint.h>

using bf16x8 = __attribute__((ext_vector_type(8))) short;
using f32x16 = __attribute__((ext_vector_type(16))) float;
using u32x4 = __attribute__((ext_vector_type(4))) unsigned;
using f32x2 = __attribute__((ext_vector_type(2))) float;
using bf2 = __attribute__((ext_vector_type(2))) __bf16;
#if defined(__HIP_DEVICE_COMPILE__)
#define NOPK __attribute__((target("no-packed-fp32-ops")))
#else
#define NOPK
#endif
#define DI __device__ __forceinline__ NOPK
#define MFMA(a, b, c) __builtin_amdgcn_mfma_f32_32x32x16_bf16(__builtin_bit_cast(bf16x8, (a)), __builtin_bit_cast(bf16x8, (b)), (c), 0, 0, 0)
#define SB() __builtin_amdgcn_sched_barrier(0)
#define PIN(x) asm volatile("" : "+v"(x))
#define STORE_AUX 16
#define CODEPF 32768

constexpr int CH = 96, HH = 224, WW = 224;
constexpr int ROWB = CH * 2;
constexpr int FRAMEB = HH * WW * ROWB;
constexpr int IMGROWB = WW * ROWB;
constexpr int NWIN = 28;
constexpr int NUNITS = NWIN * NWIN;
constexpr int WMAT_BYTES = 18 * 1024;
constexpr float LN_EPS = 1e-5f;
constexpr int NWG = 256;
constexpr float QK_SCALE = 0.10206207261596575f * 1.4426950408889634f;

template <int I> struct IC { static constexpr int v = I; };
template <int B, int E, class F> DI void sfor(F&& f) { if constexpr (B < E) { f(IC<B>{}); sfor<B + 1, E>(f); } }
#define LAM(M) [&](auto M) __attribute__((always_inline)) NOPK

DI unsigned pk2(float a, float b) {
  f32x2 v = {a, b};
  return __builtin_bit_cast(unsigned, __builtin_convertvector(v, bf2));
}
DI float lo16(unsigned u) { return __builtin_bit_cast(float, u << 16); }
DI float hi16(unsigned u) { return __builtin_bit_cast(float, u & 0xffff0000u); }
DI unsigned fbits(float f) { return __builtin_bit_cast(unsigned, f); }
DI float bitsf(unsigned u) { return __builtin_bit_cast(float, u); }
DI float xhalf_sum(float v) {
  auto r = __builtin_amdgcn_permlane32_swap(fbits(v), fbits(v), false, false);
  return bitsf(r[0]) + bitsf(r[1]);
}
DI float xhalf_max(float v) {
  auto r = __builtin_amdgcn_permlane32_swap(fbits(v), fbits(v), false, false);
  return __builtin_fmaxf(bitsf(r[0]), bitsf(r[1]));
}
DI float max3(float a, float b, float c) { return __builtin_fmaxf(__builtin_fmaxf(a, b), c); }
DI u32x4 pack8(const f32x16& a, int s) {
  u32x4 r;
  r[0] = pk2(a[8 * s + 0], a[8 * s + 1]);
  r[1] = pk2(a[8 * s + 2], a[8 * s + 3]);
  r[2] = pk2(a[8 * s + 4], a[8 * s + 5]);
  r[3] = pk2(a[8 * s + 6], a[8 * s + 7]);
  return r;
}

typedef __attribute__((address_space(3))) char lds_char;
typedef __attribute__((address_space(3))) unsigned lds_u32;
DI u32x4 lds_ld(const lds_char* p) { return *(const __attribute__((address_space(3))) u32x4*)p; }
DI void lds_st(lds_char* p, u32x4 v) { *(__attribute__((address_space(3))) u32x4*)p = v; }

constexpr int LDS_CNT = 0;
DI void cnt_arrive(const lds_char* l16, unsigned dvoff, int k) {
  asm volatile("s_lshl_b64 exec, 1, %2\n\tds_add_u32 %0, %1 offset:%3\n\ts_mov_b64 exec, -1"
               :: "v"((unsigned)(size_t)l16), "v"(dvoff), "s"(k), "i"(LDS_CNT) : "memory", "scc");
}
DI unsigned cnt_peek(const lds_char* l16) {
  return __hip_atomic_load((const lds_u32*)(l16 + LDS_CNT), __ATOMIC_RELAXED, __HIP_MEMORY_SCOPE_WORKGROUP);
}
DI void cnt_wait(const lds_char* l16, int k, unsigned n, unsigned peeked) {
  const unsigned target = n * 16u * (unsigned)k;
  asm volatile("" ::: "memory");
  if (__builtin_expect((unsigned)__builtin_amdgcn_readlane(peeked, k) < target, 0)) {
    for (;;) {
      __builtin_amdgcn_s_sleep(1);
      if ((unsigned)__builtin_amdgcn_readlane(cnt_peek(l16), k) >= target) break;
    }
  }
  asm volatile("" ::: "memory");
}
DI void cnt_wait(const lds_char* l16, int k, unsigned n) { cnt_wait(l16, k, n, cnt_peek(l16)); }

typedef __attribute__((address_space(1))) unsigned gu32;
constexpr int SCR_OFF = 65536;
constexpr int XFS = 1552;
constexpr int XREG = 6304;
constexpr int OSTG = 4096;
constexpr int LDS_ID = 256;
constexpr int LDS_W0 = LDS_ID + 2048;
constexpr int LDS_X = LDS_W0 + 2 * WMAT_BYTES;
constexpr int LDS_W2 = LDS_X + 8 * XREG;
constexpr int LDS_O = LDS_W2 + 2 * WMAT_BYTES;
constexpr int LDS_RAW = LDS_W2;
constexpr int LDS_RAW_END = LDS_RAW + 73728;
constexpr int LDS_TOTAL = LDS_RAW_END;
static_assert(LDS_O + 8 * OSTG <= LDS_RAW_END && LDS_O + 2 * WMAT_BYTES <= LDS_RAW_END, "staging");
static_assert(LDS_TOTAL <= 163840, "lds");

DI void dma_tile(const char* row, unsigned voff, unsigned lds) {
  const char* r1 = row + FRAMEB; const char* r2 = row + 2 * (size_t)FRAMEB; const char* r3 = row + 3 * (size_t)FRAMEB;
  unsigned keep;
  asm volatile(
      "s_mov_b32 %0, m0\n\t"
      "s_mov_b32 m0, %6\n\ts_nop 0\n\t"
      "global_load_lds_dwordx4 %1, %2\n\t"
      "s_add_u32 m0, m0, %7\n\ts_nop 0\n\t"
      "global_load_lds_dwordx4 %1, %3\n\t"
      "s_add_u32 m0, m0, %7\n\ts_nop 0\n\t"
      "global_load_lds_dwordx4 %1, %4\n\t"
      "s_add_u32 m0, m0, %7\n\ts_nop 0\n\t"
      "global_load_lds_dwordx4 %1, %5\n\t"
      "s_mov_b32 exec_hi, 0\n\t"
      "global_load_lds_dwordx4 %1, %5 offset:1024\n\t"
      "s_sub_u32 m0, m0, %7\n\ts_nop 0\n\t"
      "global_load_lds_dwordx4 %1, %4 offset:1024\n\t"
      "s_sub_u32 m0, m0, %7\n\ts_nop 0\n\t"
      "global_load_lds_dwordx4 %1, %3 offset:1024\n\t"
      "s_sub_u32 m0, m0, %7\n\ts_nop 0\n\t"
      "global_load_lds_dwordx4 %1, %2 offset:1024\n\t"
      "s_mov_b32 exec_hi, -1\n\t"
      "s_mov_b32 m0, %0"
      : "=&s"(keep) : "v"(voff), "s"(row), "s"(r1), "s"(r2), "s"(r3), "s"(lds), "i"(XFS) : "memory", "scc");
}
DI void dma_tile_sc1(const char* row, unsigned voff, unsigned lds) {
  const char* r1 = row + FRAMEB; const char* r2 = row + 2 * (size_t)FRAMEB; const char* r3 = row + 3 * (size_t)FRAMEB;
  unsigned keep;
  asm volatile(
      "s_mov_b32 %0, m0\n\t"
      "s_mov_b32 m0, %6\n\ts_nop 0\n\t"
      "global_load_lds_dwordx4 %1, %2 sc1\n\t"
      "s_add_u32 m0, m0, %7\n\ts_nop 0\n\t"
      "global_load_lds_dwordx4 %1, %3 sc1\n\t"
      "s_add_u32 m0, m0, %7\n\ts_nop 0\n\t"
      "global_load_lds_dwordx4 %1, %4 sc1\n\t"
      "s_add_u32 m0, m0, %7\n\ts_nop 0\n\t"
      "global_load_lds_dwordx4 %1, %5 sc1\n\t"
      "s_mov_b32 exec_hi, 0\n\t"
      "global_load_lds_dwordx4 %1, %5 offset:1024 sc1\n\t"
      "s_sub_u32 m0, m0, %7\n\ts_nop 0\n\t"
      "global_load_lds_dwordx4 %1, %4 offset:1024 sc1\n\t"
      "s_sub_u32 m0, m0, %7\n\ts_nop 0\n\t"
      "global_load_lds_dwordx4 %1, %3 offset:1024 sc1\n\t"
      "s_sub_u32 m0, m0, %7\n\ts_nop 0\n\t"
      "global_load_lds_dwordx4 %1, %2 offset:1024 sc1\n\t"
      "s_mov_b32 exec_hi, -1\n\t"
      "s_mov_b32 m0, %0"
      : "=&s"(keep) : "v"(voff), "s"(row), "s"(r1), "s"(r2), "s"(r3), "s"(lds), "i"(XFS) : "memory", "scc");
}
DI void dma_piece(const char* base, unsigned voff, unsigned lds) {
  unsigned keep;
  asm volatile("s_mov_b32 %0, m0\n\ts_mov_b32 m0, %3\n\ts_nop 0\n\tglobal_load_lds_dwordx4 %1, %2\n\ts_mov_b32 m0, %0"
               : "=&s"(keep) : "v"(voff), "s"(base), "s"(lds) : "memory");
}
#define VMWAIT(n) asm volatile("s_waitcnt vmcnt(" #n ")" ::: "memory")
#define LGWAIT() asm volatile("s_waitcnt lgkmcnt(0)" ::: "memory")

template <int CPR, int SS>
DI void raw_piece(const char* src, int piece, int lane, unsigned lds_img) {
  const unsigned q = (unsigned)(piece * 64 + lane);
  const unsigned row = (q * (unsigned)(((1 << 20) + CPR - 1) / CPR)) >> 20;
  const unsigned off = (SS == CPR * 16) ? 16u * (q ^ ((row >> 2) & 3u)) : row * (unsigned)(SS - CPR * 16) + 16u * (q ^ ((row >> 2) & 3u));
  dma_piece(src, off, lds_img + piece * 1024);
}
typedef short s16x4 __attribute__((ext_vector_type(4)));
DI void fold_read_rows(u32x4* d, const lds_char* img, int rb, int row0, int sw, int hh) {
  const int x = (sw >> 2) & 3;
  const lds_char* b0 = img + (row0 + sw) * rb + 16 * (hh ^ x);
  const lds_char* b1 = img + (row0 + sw) * rb + 16 * ((2 + hh) ^ x);
#pragma unroll
  for (int s = 0; s < 6; ++s) d[s] = lds_ld(((s & 1) ? b1 : b0) + 64 * (s >> 1));
}
DI void fold_read_cols(u32x4* d, const lds_char* img, int T, int lane) {
  const int hh = lane >> 5, cb = (lane >> 4) & 1, qq = (lane >> 2) & 3, pp = lane & 3;
  const int cc = 4 * T + 2 * cb + (pp & 1);
  const lds_char* b0 = img + (8 * hh + qq) * 192 + 16 * (cc ^ ((2 * hh) & 3)) + 8 * (pp >> 1);
  const lds_char* b1 = img + (8 * hh + qq + 4) * 192 + 16 * (cc ^ ((2 * hh + 1) & 3)) + 8 * (pp >> 1);
#pragma unroll
  for (int s = 0; s < 6; ++s) {
    s16x4 lo = __builtin_amdgcn_ds_read_tr16_b64_v4i16((__attribute__((address_space(3))) s16x4*)(b0 + s * 16 * 192));
    s16x4 hi = __builtin_amdgcn_ds_read_tr16_b64_v4i16((__attribute__((address_space(3))) s16x4*)(b1 + s * 16 * 192));
    d[s] = __builtin_bit_cast(u32x4, __builtin_shufflevector(lo, hi, 0, 1, 2, 3, 4, 5, 6, 7));
  }
}
DI void fold_compute(const u32x4 (&a)[18], const u32x4 (&b)[6], float sc, u32x4 (&res)[6]) {
  const f32x16 zero = {};
  f32x16 acc[3];
#pragma unroll
  for (int s = 0; s < 6; ++s)
#pragma unroll
    for (int Tin = 0; Tin < 3; ++Tin) acc[Tin] = MFMA(a[Tin * 6 + s], b[s], s == 0 ? zero : acc[Tin]);
#pragma unroll
  for (int Tin = 0; Tin < 3; ++Tin) {
#pragma unroll
    for (int i = 0; i < 16; ++i) acc[Tin][i] *= sc;
    res[2 * Tin] = pack8(acc[Tin], 0);
    res[2 * Tin + 1] = pack8(acc[Tin], 1);
  }
}
DI void fold_store(lds_char* wdst, const u32x4 (&res)[6]) {
#pragma unroll
  for (int s = 0; s < 6; ++s) lds_st(wdst + s * 1024, res[s]);
}

struct Stats { float s0, s1, q0, q1, rs, nb; };
DI void stat_pkt(const u32x4 (&xf)[6], f32x16 (&xs)[3], Stats& st, int k) {
  const int s = k >> 2, i = k & 3, T = s >> 1, e = 8 * (s & 1) + 2 * i;
  const float a = lo16(xf[s][i]), b = hi16(xf[s][i]);
  xs[T][e] = a; xs[T][e + 1] = b;
  if (k == 0) { st.s0 = a; st.s1 = b; st.q0 = a * a; st.q1 = b * b; }
  else { st.s0 += a; st.s1 += b; st.q0 = __builtin_fmaf(a, a, st.q0); st.q1 = __builtin_fmaf(b, b, st.q1); }
  PIN(st.s0); PIN(st.s1); PIN(st.q0); PIN(st.q1);
}
DI void stat_fin(Stats& st) {
  float s = xhalf_sum(st.s0 + st.s1), q = xhalf_sum(st.q0 + st.q1);
  float m = s * (1.f / CH);
  float var = __builtin_fmaxf(__builtin_fmaf(-m, m, q * (1.f / CH)), 0.f);
  st.rs = __builtin_amdgcn_rsqf(var + LN_EPS);
  st.nb = -m * st.rs;
  PIN(st.rs); PIN(st.nb);
}
DI void nrm_pkt(const f32x16 (&xs)[3], u32x4 (&hf)[6], const Stats& st, int k) {
  const int s = k >> 2, i = k & 3, T = s >> 1, e = 8 * (s & 1) + 2 * i;
  unsigned v = pk2(__builtin_fmaf(xs[T][e], st.rs, st.nb), __builtin_fmaf(xs[T][e + 1], st.rs, st.nb));
  PIN(v);
  hf[s][i] = v;
}
DI void cvt4(u32x4& d, const f32x16& a, int s) { d = pack8(a, s); PIN(d); }

constexpr int RD = 4;
constexpr int NWSEQ = 144;
DI constexpr int woff(int n) {
  n %= NWSEQ;
  if (n < 72) {
    int k = n % 36;
    if (k < 12) return LDS_W0 + (k & 1) * WMAT_BYTES + (k >> 1) * 1024;
    if (k < 24) return LDS_W0 + (6 + (k - 12)) * 1024;
    return LDS_W0 + WMAT_BYTES + (6 + (k - 24)) * 1024;
  }
  if (n < 108) return LDS_W2 + ((n - 72) % 18) * 1024;
  return LDS_W2 + WMAT_BYTES + (((n - 108) / 12) * 6 + (n - 108) % 6) * 1024;
}
constexpr int WB_BASE = LDS_W2;
struct Ctx {
  const lds_char* wa;
  const lds_char* wb;
  bool ms0, ms1, mg[4];
};
DI u32x4 wload(const Ctx& c, int n) {
  const int o = woff(n);
  return o < LDS_W2 ? lds_ld(c.wa + o) : lds_ld(c.wb + (o - WB_BASE));
}
#define WUSE(n) ring[(n) % RD]
#define WNEXT(n) ring[(n) % RD] = wload(c, (n) + RD)

struct Slot { int ty, T, s; };
DI constexpr Slot tslot(int m) {
  if (m < 12) return {m & 1, 0, m >> 1};
  if (m < 15) return {0, 1, m - 12};
  if (m < 17) return {2, 0, m - 15};
  if (m < 20) return {0, 1, m - 14};
  if (m < 23) return {0, 2, m - 20};
  if (m < 25) return {2, 1, m - 23};
  if (m < 28) return {0, 2, m - 22};
  if (m < 31) return {1, 1, m - 28};
  if (m < 33) return {2, 2, m - 31};
  if (m < 36) return {1, 1, m - 30};
  if (m < 42) return {1, 2, m - 36};
  return {3, (m - 42) >> 1, (m - 42) & 1};
}
DI constexpr int twidx(int m) {
  return m < 15 ? m : (m < 17 ? 15 : (m < 23 ? m - 2 : (m < 25 ? 21 : (m < 31 ? m - 4 : (m < 33 ? 27 : (m < 42 ? m - 6 : 36))))));
}
DI constexpr int sched_cnt(int m, int first, int last, int total) {
  return m <= first ? 0 : (m >= last ? total : (m - first) * total / (last - first));
}

struct TState { f32x16 g[3], v[3], sa; u32x4 hf[6], gp[2], vp[3][2], pf[2]; float e[4]; unsigned pk0, pk1; };
template <int K0, int K1>
DI void stat_pkts(const u32x4 (&xf)[6], f32x16 (&xs)[3], Stats& st) {
  if constexpr (K0 < K1) { stat_pkt(xf, xs, st, K0); stat_pkts<K0 + 1, K1>(xf, xs, st); }
}
template <int N0, int m, int M1, int HS, bool PE, bool OE>
DI void temporal_slots(const Ctx& c, u32x4 (&ring)[RD], TState& t, f32x16 (&xs)[3], const Stats& st,
                       const u32x4 (&nx)[6], f32x16 (&nxs)[3], Stats& nst, u32x4 (&nhf)[6], const f32x16 (&pxs)[3], lds_char* xw) {
  const f32x16 zero = {};
  if constexpr (m < M1) {
    constexpr Slot sl = tslot(m);
    constexpr int n = N0 + twidx(m);
    if constexpr (sl.ty == 0) { t.g[sl.T] = MFMA(WUSE(n), t.hf[sl.s], sl.s == 0 ? zero : t.g[sl.T]); WNEXT(n); }
    else if constexpr (sl.ty == 1) { t.v[sl.T] = MFMA(t.hf[sl.s], WUSE(n), sl.s == 0 ? zero : t.v[sl.T]); WNEXT(n); }
    else if constexpr (sl.ty == 2) t.sa = MFMA(t.hf[2 * sl.T + sl.s], t.gp[sl.s], (sl.T == 0 && sl.s == 0) ? zero : t.sa);
    else xs[sl.T] = MFMA(t.vp[sl.T][sl.s], t.pf[sl.s], xs[sl.T]);
    SB();
    if constexpr (m < 10) { nrm_pkt(xs, t.hf, st, 4 + 2 * m); nrm_pkt(xs, t.hf, st, 5 + 2 * m); }
    if constexpr (PE && m >= 2 && m < 8) { u32x4 a; cvt4(a, pxs[(m - 2) >> 1], m & 1); lds_st(xw + (m - 2) * 1024, a); }
    if constexpr (m == 12) cvt4(t.gp[0], t.g[0], 0);
    if constexpr (m == 13) cvt4(t.gp[1], t.g[0], 1);
    if constexpr (m == 14) cvt4(t.vp[0][0], t.v[0], 0);
    if constexpr (m == 15) cvt4(t.vp[0][1], t.v[0], 1);
    if constexpr (m == 21) { cvt4(t.gp[0], t.g[1], 0); }
    if constexpr (m == 22) { cvt4(t.gp[1], t.g[1], 1); }
    if constexpr (m == 29) { cvt4(t.gp[0], t.g[2], 0); }
    if constexpr (m == 30) { cvt4(t.gp[1], t.g[2], 1); }
    if constexpr (m == 35 || m == 36) {
#pragma unroll
      for (int k = 2 * (m - 35); k < 2 * (m - 35) + 2; ++k) {
        float s1 = t.sa[4 + k], s3 = t.sa[12 + k];
        asm("" : "+v"(s1)); asm("" : "+v"(s3));
        float a = c.ms0 ? s1 : t.sa[k];
        float b = c.ms0 ? s3 : t.sa[8 + k];
        t.e[k] = c.ms1 ? b : a; PIN(t.e[k]);
      }
    }
    if constexpr (m == 37) {
      float mx = max3(max3(t.e[0], t.e[1], t.e[2]), t.e[3], t.e[3]);
#pragma unroll
      for (int k = 0; k < 4; ++k) { t.e[k] = __builtin_amdgcn_exp2f(t.e[k] - mx); PIN(t.e[k]); }
    }
    if constexpr (m == 38) {
      float rinv = __builtin_amdgcn_rcpf((t.e[0] + t.e[1]) + (t.e[2] + t.e[3]));
      t.pk0 = pk2(t.e[0] * rinv, t.e[1] * rinv); t.pk1 = pk2(t.e[2] * rinv, t.e[3] * rinv);
      PIN(t.pk0); PIN(t.pk1);
    }
    if constexpr (m == 39) {
#pragma unroll
      for (int g = 0; g < 4; ++g) {
        t.pf[g >> 1][(g & 1) * 2 + 0] = c.mg[g] ? t.pk0 : 0u;
        t.pf[g >> 1][(g & 1) * 2 + 1] = c.mg[g] ? t.pk1 : 0u;
      }
      PIN(t.pf[0]); PIN(t.pf[1]);
    }
    if constexpr (m == 40) { cvt4(t.vp[1][0], t.v[1], 0); cvt4(t.vp[1][1], t.v[1], 1); }
    if constexpr (m == 44) { cvt4(t.vp[2][0], t.v[2], 0); cvt4(t.vp[2][1], t.v[2], 1); }
    if constexpr (m < 35) stat_pkts<sched_cnt(m, HS + 2, 35, 20), sched_cnt(m + 1, HS + 2, 35, 20)>(nx, nxs, nst);
    if constexpr (m == 41) stat_pkts<20, 22>(nx, nxs, nst);
    if constexpr (m == 42) stat_pkts<22, 24>(nx, nxs, nst);
    if constexpr (m == 43) stat_fin(nst);
    if constexpr (m == 45) { nrm_pkt(nxs, nhf, nst, 0); nrm_pkt(nxs, nhf, nst, 1); }
    if constexpr (m == 46) { nrm_pkt(nxs, nhf, nst, 2); nrm_pkt(nxs, nhf, nst, 3); }
    if constexpr (OE && m == 46) { u32x4 a; cvt4(a, xs[0], 0); lds_st(xw, a); }
    if constexpr (OE && m == 47) { u32x4 a; cvt4(a, xs[0], 1); lds_st(xw + 1024, a); }
    SB();
    temporal_slots<N0, m + 1, M1, HS, PE, OE>(c, ring, t, xs, st, nx, nxs, nst, nhf, pxs, xw);
  }
}

struct SMState { float m0, m1, mx, l0, l1, rinv; };
template <int u>
DI void sm_unit(f32x16& a, f32x16& b, SMState& s, u32x4 (&pf)[4]) {
  if constexpr (u < 4) {
    f32x16& x = (u < 2) ? a : b;
    const int o = 8 * (u & 1);
    if (u == 0) { s.m0 = max3(x[0], x[1], x[2]); s.m1 = max3(x[3], x[4], x[5]); s.m0 = max3(s.m0, x[6], x[7]); }
    else { s.m0 = max3(s.m0, x[o], x[o + 1]); s.m1 = max3(s.m1, x[o + 2], x[o + 3]); s.m0 = max3(s.m0, x[o + 4], x[o + 5]); s.m1 = max3(s.m1, x[o + 6], x[o + 7]); }
    PIN(s.m0); PIN(s.m1);
  } else if constexpr (u == 4) {
    s.mx = xhalf_max(__builtin_fmaxf(s.m0, s.m1));
    if (__builtin_expect(__builtin_amdgcn_ballot_w64(!(__builtin_fabsf(s.mx) <= 40.f)) != 0ull, 0)) {
#pragma unroll
      for (int i = 0; i < 16; ++i) { a[i] -= s.mx; b[i] -= s.mx; }
    }
  } else if constexpr (u < 21) {
    constexpr int k = u - 5;
    f32x16& x = (k < 8) ? a : b;
    constexpr int o = 2 * (k & 7);
    float e0 = __builtin_amdgcn_exp2f(x[o]), e1 = __builtin_amdgcn_exp2f(x[o + 1]);
    PIN(e0); PIN(e1);
    x[o] = e0; x[o + 1] = e1;
    if constexpr (k >= 2) {
      f32x16& y = (k - 1 < 8) ? a : b;
      constexpr int o1 = 2 * ((k - 1) & 7);
      if constexpr (k == 2) { s.l0 = a[0] + y[o1]; s.l1 = a[1] + y[o1 + 1]; } else { s.l0 += y[o1]; s.l1 += y[o1 + 1]; }
      PIN(s.l0); PIN(s.l1);
    }
  } else if constexpr (u == 21) {
    s.rinv = __builtin_amdgcn_rcpf(xhalf_sum((s.l0 + b[14]) + (s.l1 + b[15]))); PIN(s.rinv);
  } else {
    const int k = u - 22;
    f32x16& x = (k < 4) ? a : b;
    const int o = 4 * (k & 3);
    unsigned p0 = pk2(x[o] * s.rinv, x[o + 1] * s.rinv), p1 = pk2(x[o + 2] * s.rinv, x[o + 3] * s.rinv);
    PIN(p0); PIN(p1);
    pf[k >> 1][2 * (k & 1)] = p0; pf[k >> 1][2 * (k & 1) + 1] = p1;
  }
}

__global__ __launch_bounds__(512, 2) NOPK void swin_kernel(const char* __restrict__ x, const char* __restrict__ wqkv_t,
                                                           const char* __restrict__ wo_t, const char* __restrict__ wqkv_s,
                                                           const char* __restrict__ wo_s, char* __restrict__ out,
                                                           char* __restrict__ ws) {
  extern __shared__ __attribute__((aligned(16))) char smem_[];
  lds_char* smem = (lds_char*)smem_;
  const unsigned smem_a = (unsigned)(size_t)smem;
  const int tid = threadIdx.x, lane = tid & 63;
  const int wid8 = __builtin_amdgcn_readfirstlane(tid >> 6);
  const int grp = wid8 >> 2, wid = wid8 & 3;
  const int r = lane & 31, hh = lane >> 5;
  const int p = r >> 2, f = r & 3;
  const int sw = (r & 0x13) | ((r & 4) << 1) | ((r & 8) >> 1);
  const unsigned dvoff = lane * 16;
  const unsigned xr_a = smem_a + LDS_X + wid8 * XREG;

  constexpr int FULL = 0, SONLY = 2, NONE = 3;
  const int bid = blockIdx.x;
  const int xj = bid >> 3, ub = (bid & 7) * 98;
  const bool shelp = xj == 8 || xj == 9, thelp = xj < 8 && grp == 0;
  const int li = shelp ? 2 * (xj - 8) + grp : (xj >> 1) & 3, ti = xj & 1;
  const int lunit = ub + 94 + li, hslot = ((bid & 7) * 4 + li) * 4 + wid;
  const int jj = xj < 8 ? xj : xj - 2;
  int unit = shelp ? ub + 90 + li : ub + jj + 30 * grp;
  int nmode = shelp ? SONLY : (grp == 0 ? FULL : NONE), nunit = shelp ? lunit : ub + jj + 60;
  const lds_char* l16 = smem + lane * 16;
  constexpr int C_TW = 5, C_MS = 6, C_RAW = 7, C_RDT = 8;
  constexpr int IMG = WMAT_BYTES;
  gu32* hfl = (gu32*)(ws + hslot * 256);
  unsigned hev, cpf = 0, hepoch;
  {
    {
      unsigned long long pc; asm volatile("s_getpc_b64 %0" : "=s"(pc));
      const char* cb = (const char*)(pc & ~255ull);
      const unsigned o = (unsigned)(((xj & 7) * 8 + wid8) * (CODEPF / 64) + (lane * 64 < CODEPF / 64 ? lane * 64 : 0));
      asm volatile("global_load_dword %0, %1, %2" : "+v"(cpf) : "v"(o), "s"(cb) : "memory");
    }
    asm volatile("global_load_dword %0, %1, %2 sc1" : "=v"(hev) : "v"(0u), "s"(hfl) : "memory");
    {
      int pc = (wid8 * 7 + xj * 5) % 54;
#pragma nounroll
      for (int i = 0; i < 7; ++i) { raw_piece<36, 576>(wqkv_t, pc, lane, smem_a + LDS_RAW); pc = pc == 53 ? 0 : pc + 1; }
      pc = (wid8 * 3 + xj) % 18;
#pragma nounroll
      for (int i = 0; i < 3; ++i) { raw_piece<12, 192>(wo_t, pc, lane, smem_a + LDS_RAW + 3 * IMG); pc = pc == 17 ? 0 : pc + 1; }
    }
    if (tid < 64) ((lds_u32*)(smem + LDS_CNT))[tid] = 0u;
    const int u0 = thelp ? lunit : unit, wy0 = u0 / NWIN, wx0 = u0 % NWIN;
    const char* x0 = x + (size_t)(wy0 * 8 + 2 * wid + (thelp ? ti : 0)) * IMGROWB + (size_t)(wx0 * 8) * ROWB;
    asm volatile("s_waitcnt vmcnt(0) lgkmcnt(0)\n\ts_barrier" : "+v"(hev), "+v"(cpf) :: "memory");
    hepoch = __builtin_amdgcn_readfirstlane(hev);
    if (grp == 0) dma_tile(x0, dvoff, xr_a);
    u32x4 fa[18], fb[6], res[6];
    auto job_read = [&](int j) __attribute__((always_inline)) NOPK {
      const lds_char* aimg = smem + LDS_RAW + (j < 3 ? 384 : 0);
#pragma unroll
      for (int Tin = 0; Tin < 3; ++Tin) fold_read_rows(fa + 6 * Tin, aimg, 576, 32 * Tin, sw, hh);
      if (j < 3) fold_read_cols(fb, smem + LDS_RAW + 3 * IMG, j, lane);
      else fold_read_rows(fb, smem + LDS_RAW + 192, 576, 32 * (j - 3), sw, hh);
    };
    if (grp == 1) {
      __builtin_amdgcn_s_setprio(1);
      const int ja = wid < 2 ? wid : 2 * wid - 2;
#pragma nounroll
      for (int stage = 0; stage < 2; ++stage) {
        lds_char* wdst = smem + (stage ? LDS_W2 : LDS_W0) + lane * 16;
        u32x4 res2[6];
        job_read(ja);
        fold_compute(fa, fb, ja < 3 ? 1.f : QK_SCALE, res);
        if (stage == 0) fold_store(wdst + (ja < 3 ? IMG : 0) + (ja % 3) * 6144, res);
        if (wid >= 2) {
          job_read(ja + 1);
          cnt_arrive(l16, dvoff, C_RDT);
          fold_compute(fa, fb, QK_SCALE, res2);
        } else cnt_arrive(l16, dvoff, C_RDT);
        if (stage == 0) {
          if (wid >= 2) fold_store(wdst + ((ja + 1) % 3) * 6144, res2);
          cnt_arrive(l16, dvoff, C_TW);
          cnt_wait(l16, C_RDT, 4);
          int pc = (wid * 14 + xj * 5) % 54;
#pragma nounroll
          for (int i = 0; i < 14; ++i) { raw_piece<36, 576>(wqkv_s, pc, lane, smem_a + LDS_RAW); pc = pc == 53 ? 0 : pc + 1; }
          pc = (wid * 5 + xj) % 18;
#pragma nounroll
          for (int i = 0; i < 5; ++i) { raw_piece<12, 192>(wo_s, pc, lane, smem_a + LDS_RAW + 3 * IMG); pc = pc == 17 ? 0 : pc + 1; }
          dma_tile(x0, dvoff, xr_a);
          VMWAIT(8);
          cnt_arrive(l16, dvoff, C_RAW);
          cnt_wait(l16, C_RAW, 4);
        } else {
          cnt_wait(l16, C_RDT, 8);
          fold_store(wdst + (ja < 3 ? IMG : 0) + (ja % 3) * 6144, res);
          if (wid >= 2) fold_store(wdst + ((ja + 1) % 3) * 6144, res2);
          cnt_arrive(l16, dvoff, C_MS);
        }
      }
      __builtin_amdgcn_s_setprio(0);
    } else if (wid8 == 3) {
      u32x4 idf[2];
#pragma unroll
      for (int s = 0; s < 2; ++s)
#pragma unroll
        for (int d = 0; d < 4; ++d)
          idf[s][d] = (s == (sw >> 4) && hh == ((sw >> 3) & 1) && d == ((sw & 7) >> 1)) ? (0x3F80u << (16 * (sw & 1))) : 0u;
      lds_st(smem + LDS_ID + lane * 16, idf[0]); lds_st(smem + LDS_ID + 1024 + lane * 16, idf[1]);
      cnt_arrive(l16, dvoff, C_MS);
    }
  }

  const int cW = 1 + 2 * grp, cR = 2 + 2 * grp;
  auto laddr = [&](unsigned off) __attribute__((always_inline)) NOPK { off += smem_a; asm("" : "+v"(off)); return (lds_char*)(size_t)off; };
  const unsigned xg = LDS_X + grp * 4 * XREG;
  lds_char* xw = laddr(xg + f * XREG + (hh * 32 + 8 * wid + p) * 16);
  const lds_char* xr = laddr(xg + wid * XREG + lane * 16);
  const lds_char* xl = laddr(xg + wid * XREG + f * XFS + p * ROWB + hh * 16);
  lds_char* ow0 = laddr(LDS_O + wid8 * OSTG + r * 64 + 16 * (hh ^ ((r >> 1) & 3)));
  lds_char* ow1 = laddr(LDS_O + wid8 * OSTG + r * 64 + 16 * ((2 + hh) ^ ((r >> 1) & 3)));
  const lds_char* ord = laddr(LDS_O + wid8 * OSTG + lane * 16);
  const f32x16 zero = {};
  Ctx c;
  c.wa = l16; c.wb = laddr(WB_BASE + lane * 16);
  {
    c.ms0 = (p >> 1) & 1;
    c.ms1 = (p >> 2) & 1;
    const bool valid = (hh == (p & 1));
#pragma unroll
    for (int g = 0; g < 4; ++g) c.mg[g] = valid && (p >> 1) == g;
  }
  unsigned ooff = (lane >> 5) * 2 * IMGROWB + ((lane >> 2) & 7) * ROWB + 16 * ((lane & 3) ^ ((lane >> 3) & 3));
  asm("" : "+v"(ooff));
  auto orsrc = __builtin_amdgcn_make_buffer_rsrc((void*)out, 0, 4 * FRAMEB, 0x00020000);

  f32x16 xsA[3], xsB[3], xsC[3]; Stats stA, stB, stC; u32x4 hfA[6];
  u32x4 x1[2][6], h2[2][6];
  u32x4 ring[RD];
  bool first = true, cold = true;
  unsigned ph = 0, pk;
  const char* nxu; int nstep; bool more;

  auto cold_start2 = [&]() __attribute__((always_inline)) NOPK {
    const int wy = unit / NWIN, wx = unit % NWIN;
    u32x4 xin[6];
#pragma unroll
    for (int s = 0; s < 6; ++s) xin[s] = lds_ld(xl + 32 * s);
    LGWAIT();
    dma_tile(x + (size_t)(wy * 8 + 2 * wid + (thelp ? 0 : 1)) * IMGROWB + (size_t)(wx * 8) * ROWB, dvoff, xr_a);
    stat_pkts<0, 24>(xin, xsA, stA);
    stat_fin(stA);
#pragma unroll
    for (int k = 0; k < 4; ++k) nrm_pkt(xsA, hfA, stA, k);
    cnt_wait(l16, C_TW, 4);
#pragma unroll
    for (int n = 0; n < RD; ++n) ring[n] = wload(c, n);
  };
  auto next_setup = [&]() __attribute__((always_inline)) NOPK {
    more = nmode != NONE;
    if (nmode == SONLY) { nxu = ws + SCR_OFF + (size_t)(hslot * 2) * 1536; nstep = 1536; }
    else { const int ny = nunit / NWIN, nx = nunit % NWIN; nxu = x + (size_t)(ny * 8 + 2 * wid) * IMGROWB + (size_t)(nx * 8) * ROWB; nstep = IMGROWB; }
  };
  auto phaseT = [&]() __attribute__((always_inline)) NOPK {
    TState t2;
    {
      TState t; u32x4 xin[6];
      t.hf[0] = hfA[0];
      temporal_slots<0, 0, 11, 10, false, false>(c, ring, t, xsA, stA, xin, xsB, stB, t2.hf, xsA, xw);
      if (cold) { VMWAIT(0); } else { VMWAIT(10); }
      if (cold && thelp && lane == 0)
        __hip_atomic_store(hfl + 32 + 16 * ti, hepoch + 1u, __ATOMIC_RELAXED, __HIP_MEMORY_SCOPE_AGENT);
#pragma unroll
      for (int s = 0; s < 6; ++s) xin[s] = lds_ld(xl + 32 * s);
      cnt_arrive(l16, dvoff, cR);
      temporal_slots<0, 11, 46, 10, false, false>(c, ring, t, xsA, stA, xin, xsB, stB, t2.hf, xsA, xw);
      pk = cnt_peek(l16);
      temporal_slots<0, 46, 48, 10, false, false>(c, ring, t, xsA, stA, xin, xsB, stB, t2.hf, xsA, xw);
    }
    {
      temporal_slots<36, 0, 2, 19, true, true>(c, ring, t2, xsB, stB, x1[0], xsC, stC, h2[0], xsA, xw);
      cnt_wait(l16, cR, ph + 4, pk);
      temporal_slots<36, 2, 8, 19, true, true>(c, ring, t2, xsB, stB, x1[0], xsC, stC, h2[0], xsA, xw);
      cnt_arrive(l16, dvoff, cW);
      temporal_slots<36, 8, 17, 19, true, true>(c, ring, t2, xsB, stB, x1[0], xsC, stC, h2[0], xsA, xw);
      pk = cnt_peek(l16);
      temporal_slots<36, 17, 20, 19, true, true>(c, ring, t2, xsB, stB, x1[0], xsC, stC, h2[0], xsA, xw);
      cnt_wait(l16, cW, ph + 4, pk);
#pragma unroll
      for (int s = 0; s < 6; ++s) x1[0][s] = lds_ld(xr + s * 1024);
      cnt_arrive(l16, dvoff, cR);
      if (first) cnt_wait(l16, C_MS, 5);
      temporal_slots<36, 20, 43, 19, true, true>(c, ring, t2, xsB, stB, x1[0], xsC, stC, h2[0], xsA, xw);
      pk = cnt_peek(l16);
      temporal_slots<36, 43, 46, 19, true, true>(c, ring, t2, xsB, stB, x1[0], xsC, stC, h2[0], xsA, xw);
      cnt_wait(l16, cR, ph + 8, pk);
      temporal_slots<36, 46, 48, 19, true, true>(c, ring, t2, xsB, stB, x1[0], xsC, stC, h2[0], xsA, xw);
    }
  };
  auto phaseS = [&](auto SOC) __attribute__((always_inline)) NOPK {
    constexpr bool SO = SOC.v != 0;
    const int wy = unit / NWIN, wx = unit % NWIN;
    f32x16 sa[2][2]; u32x4 gq[6];
    {
      f32x16 g[3], xsD[3]; Stats stD;
      sfor<0, 10>(LAM(M) {
        constexpr int m = M.v, n = 72 + m, T = m / 6, s = m % 6;
        g[T] = MFMA(WUSE(n), h2[0][s], s == 0 ? zero : g[T]); WNEXT(n);
        SB();
        if constexpr (m < 2) {
          if constexpr (!SO) {
            u32x4 a, b; cvt4(a, xsB[1 + m], 0); lds_st(xw + (2 + 2 * m) * 1024, a); cvt4(b, xsB[1 + m], 1); lds_st(xw + (3 + 2 * m) * 1024, b);
            if constexpr (m == 1) cnt_arrive(l16, dvoff, cW);
          }
        }
        if constexpr (m < 5) {
#pragma unroll
          for (int k = 4 + 4 * m; k < 8 + 4 * m; ++k) nrm_pkt(xsC, h2[0], stC, k);
        }
        if constexpr (m == 7) { cvt4(gq[0], g[0], 0); cvt4(gq[1], g[0], 1); }
        if constexpr (!SO && m == 8) pk = cnt_peek(l16);
        SB();
      });
      if constexpr (!SO) {
        cnt_wait(l16, cW, ph + 8, pk);
#pragma unroll
        for (int s = 0; s < 6; ++s) x1[1][s] = lds_ld(xr + s * 1024);
      } else {
        VMWAIT(10);
#pragma unroll
        for (int s = 0; s < 6; ++s) x1[1][s] = lds_ld(xl + 32 * s);
      }
      sfor<10, 24>(LAM(M) {
        constexpr int m = M.v;
        if constexpr (m < 12) { constexpr int n = 72 + m; g[1] = MFMA(WUSE(n), h2[0][m - 6], g[1]); WNEXT(n); }
        else if constexpr (m < 14) sa[0][0] = MFMA(h2[0][m - 12], gq[m - 12], m == 12 ? zero : sa[0][0]);
        else if constexpr (m < 20) { constexpr int n = 72 + m - 2; g[2] = MFMA(WUSE(n), h2[0][m - 14], m == 14 ? zero : g[2]); WNEXT(n); }
        else sa[0][0] = MFMA(h2[0][m - 18], gq[m - 18], sa[0][0]);
        SB();
        if constexpr (m == 13) {
          LGWAIT();
          if (!SO && nmode == FULL) dma_tile(nxu, dvoff, xr_a);
        }
        if constexpr (m == 14) { cvt4(gq[2], g[1], 0); cvt4(gq[3], g[1], 1); }
        if constexpr (m == 21) { cvt4(gq[4], g[2], 0); cvt4(gq[5], g[2], 1); }
        stat_pkts<sched_cnt(m, 11, 20, 24), sched_cnt(m + 1, 11, 20, 24)>(x1[1], xsD, stD);
        if constexpr (m == 19) stat_fin(stD);
        if constexpr (m >= 20) {
#pragma unroll
          for (int k = 6 * (m - 20); k < 6 * (m - 19); ++k) nrm_pkt(xsD, h2[1], stD, k);
        }
        SB();
      });
    }
    u32x4 pf[2][4];
    {
      f32x16 g[3]; u32x4 gp[2][2]; SMState sm;
      sfor<0, 36>(LAM(M) {
        constexpr int m = M.v;
        if constexpr (m < 6) sa[0][1] = MFMA(h2[1][m], gq[m], m == 0 ? zero : sa[0][1]);
        else if constexpr (m < 18) { constexpr int n = 90 + m - 6, T = (m - 6) / 6, s = (m - 6) % 6; g[T] = MFMA(WUSE(n), h2[1][s], s == 0 ? zero : g[T]); WNEXT(n); }
        else if constexpr (m < 22) { constexpr int kt = (m - 18) & 1, s = (m - 18) >> 1; sa[1][kt] = MFMA(h2[kt][s], gp[0][s], s == 0 ? zero : sa[1][kt]); }
        else if constexpr (m < 28) { constexpr int n = 90 + m - 10, s = m - 22; g[2] = MFMA(WUSE(n), h2[1][s], s == 0 ? zero : g[2]); WNEXT(n); }
        else { constexpr int T = (m - 28) / 4 + 1, kt = (m - 28) & 1, s = ((m - 28) >> 1) & 1; sa[1][kt] = MFMA(h2[kt][2 * T + s], gp[T & 1][s], sa[1][kt]); }
        SB();
        if constexpr (m == 14) { cvt4(gp[0][0], g[0], 0); cvt4(gp[0][1], g[0], 1); }
        if constexpr (m == 20) { cvt4(gp[1][0], g[1], 0); cvt4(gp[1][1], g[1], 1); }
        if constexpr (m == 30) { cvt4(gp[0][0], g[2], 0); cvt4(gp[0][1], g[2], 1); }
        if constexpr (m == 2 || m == 3) sm_unit<m - 2>(sa[0][0], sa[0][1], sm, pf[0]);
        if constexpr (m >= 8) sm_unit<m - 6>(sa[0][0], sa[0][1], sm, pf[0]);
        SB();
      });
    }
    if (!SO && nmode == SONLY) {
      for (int spin = 0; spin < (1 << 22); ++spin) {
        const unsigned f0 = __hip_atomic_load(hfl + 32, __ATOMIC_RELAXED, __HIP_MEMORY_SCOPE_AGENT);
        const unsigned f1 = __hip_atomic_load(hfl + 48, __ATOMIC_RELAXED, __HIP_MEMORY_SCOPE_AGENT);
        if (__builtin_amdgcn_readfirstlane(((f0 - hepoch) ^ 1u) | ((f1 - hepoch) ^ 1u)) == 0u) break;
        __builtin_amdgcn_s_sleep(8);
      }
      __builtin_amdgcn_fence(__ATOMIC_ACQUIRE, "agent");
      asm volatile("" ::: "memory");
      if (lane == 0) __hip_atomic_store(hfl, hepoch + 1u, __ATOMIC_RELAXED, __HIP_MEMORY_SCOPE_AGENT);
      dma_tile_sc1(nxu, dvoff, xr_a);
    }
    {
      u32x4 vp[3][4], idf[2], xin[6], ov[2];
      f32x16 acc[12]; SMState sm;
      const unsigned obase = wid * FRAMEB + (wy * 8) * IMGROWB + (wx * 8) * ROWB;
      sfor<0, 81>(LAM(M) {
        constexpr int m = M.v, ch = m / 6, k = m % 6;
        constexpr int CT[12] = {0, 0, 0, 1, 0, 0, 1, 0, 1, 1, 1, 1};
        constexpr int CA[12] = {0, 1, 2, 0, 3, 4, 1, 5, 3, 2, 4, 5};
        if constexpr (m == 48) {
          if (!SO && more) {
            VMWAIT(2);
#pragma unroll
            for (int s = 0; s < 6; ++s) xin[s] = lds_ld(xl + 32 * s);
            LGWAIT();
            if (nmode == SONLY) dma_tile_sc1(nxu + nstep, dvoff, xr_a); else dma_tile(nxu + nstep, dvoff, xr_a);
          } else {
#pragma unroll
            for (int s = 0; s < 6; ++s) xin[s] = u32x4{};
          }
        }
        if constexpr (ch < 12) {
          if constexpr (CT[ch] == 0) {
            constexpr int vc = CA[ch], n = 108 + 6 * vc + k;
            acc[ch] = MFMA(h2[vc & 1][k], WUSE(n), k == 0 ? zero : acc[ch]); WNEXT(n);
          } else {
            constexpr int q = CA[ch] / 3, T = CA[ch] % 3;
            if constexpr (k < 2) acc[ch] = MFMA(idf[k], x1[q][2 * T + k], k == 0 ? zero : acc[ch]);
            else acc[ch] = MFMA(vp[T][k - 2], pf[q][k - 2], acc[ch]);
          }
          SB();
        }
        if constexpr (m == 12) { idf[0] = lds_ld(c.wa + LDS_ID); idf[1] = lds_ld(c.wa + LDS_ID + 1024); }
        if constexpr (m >= 2 && m < 42) {
          sfor<sched_cnt(m, 2, 42, 30), sched_cnt(m + 1, 2, 42, 30)>(LAM(U) { sm_unit<U.v>(sa[1][0], sa[1][1], sm, pf[1]); });
        }
        if constexpr (ch >= 1 && ch <= 12) {
          constexpr int pc = ch - 1;
          if constexpr (CT[pc] == 0) {
            if constexpr (k == 2 || k == 3) cvt4(vp[CA[pc] >> 1][2 * (CA[pc] & 1) + k - 2], acc[pc], k - 2);
          } else {
            constexpr int ob = (pc & 1) * 2048;
            if constexpr (k == 2) { u32x4 a; cvt4(a, acc[pc], 0); lds_st(ow0 + ob, a); }
            if constexpr (k == 3) { u32x4 a; cvt4(a, acc[pc], 1); lds_st(ow1 + ob, a); asm volatile("" ::: "memory"); }
            if constexpr (k == 4) { ov[0] = lds_ld(ord + ob); ov[1] = lds_ld(ord + ob + 1024); asm volatile("" ::: "memory"); }
          }
        }
        if constexpr (ch >= 2 && k == 1) {
          constexpr int pc = ch - 2;
          if constexpr (CT[pc] == 1) {
            constexpr int q = CA[pc] / 3, T = CA[pc] % 3;
            SB();
            __builtin_amdgcn_raw_buffer_store_b128(ov[0], orsrc, ooff, obase + q * IMGROWB + 64 * T, STORE_AUX);
            __builtin_amdgcn_raw_buffer_store_b128(ov[1], orsrc, ooff, obase + (4 + q) * IMGROWB + 64 * T, STORE_AUX);
            SB(); asm volatile("s_nop 1" ::: "memory"); SB();
          }
        }
        if constexpr (!SO && m >= 49) {
          stat_pkts<sched_cnt(m, 49, 70, 24), sched_cnt(m + 1, 49, 70, 24)>(xin, xsA, stA);
          if constexpr (m == 70) stat_fin(stA);
          if constexpr (m == 72 || m == 73) { nrm_pkt(xsA, hfA, stA, 2 * (m - 72)); nrm_pkt(xsA, hfA, stA, 2 * (m - 72) + 1); }
        }
        SB();
      });
    }
  };

  VMWAIT(0);
  if (shelp && grp == 1) __builtin_amdgcn_s_setprio(1);
  cold_start2();
  if (thelp) {
    TState t; u32x4 xin[6], hfB[6];
    t.hf[0] = hfA[0];
    temporal_slots<0, 0, 11, 10, false, false>(c, ring, t, xsA, stA, xin, xsB, stB, hfB, xsA, xw);
    VMWAIT(0);
#pragma unroll
    for (int s = 0; s < 6; ++s) xin[s] = lds_ld(xl + 32 * s);
    cnt_arrive(l16, dvoff, cR);
    temporal_slots<0, 11, 48, 10, false, false>(c, ring, t, xsA, stA, xin, xsB, stB, hfB, xsA, xw);
    cnt_wait(l16, cR, 4);
#pragma unroll
    for (int s = 0; s < 6; ++s) lds_st(xw + s * 1024, pack8(xsA[s >> 1], s & 1));
    cnt_arrive(l16, dvoff, cW);
#pragma unroll
    for (int n = 0; n < RD; ++n) ring[n] = wload(c, n);
    cnt_wait(l16, cW, 4);
#pragma unroll
    for (int s = 0; s < 6; ++s) xin[s] = lds_ld(xr + s * 1024);
#pragma unroll
    for (int s = 0; s < 6; ++s) lds_st((lds_char*)xl + 32 * s, xin[s]);
    asm volatile("" ::: "memory");
#pragma unroll
    for (int k = 0; k < 6; ++k) {
      const unsigned q = k * 64 + lane, row = (q * 683u) >> 16, n = q - 96u * row;
      xin[k] = lds_ld(smem + LDS_X + wid8 * XREG + row * XFS + 16 * n);
    }
    LGWAIT();
    {
      const int wy = unit / NWIN, wx = unit % NWIN;
      dma_tile(x + (size_t)(wy * 8 + 2 * wid + 1) * IMGROWB + (size_t)(wx * 8) * ROWB, dvoff, xr_a);
    }
    auto srs = __builtin_amdgcn_make_buffer_rsrc((void*)(ws + SCR_OFF), 0, 4 * FRAMEB, 0x00020000);
#pragma unroll
    for (int k = 0; k < 6; ++k) {
      const unsigned q = k * 64 + lane, row = (q * 683u) >> 16, n = q - 96u * row;
      __builtin_amdgcn_raw_buffer_store_b128(xin[k], srs, row * FRAMEB + 16 * n + (hslot * 2 + ti) * 1536, 0, 16);
    }
#pragma unroll
    for (int T = 0; T < 3; ++T) xsA[T] = xsB[T];
    stA = stB; hfA[0] = hfB[0];
    ph = 4;
  }
  for (;;) {
    next_setup();
    phaseT();
    phaseS(IC<0>{});
    if (nmode != FULL) break;
    ph += 8;
    unit = nunit; nmode = NONE;
    first = false; cold = false;
  }
  if (nmode == SONLY) {
    unit = nunit;
    __builtin_amdgcn_s_setprio(0);
#pragma unroll
    for (int T = 0; T < 3; ++T) xsC[T] = xsA[T];
    stC = stA; h2[0][0] = hfA[0];
#pragma unroll
    for (int s = 0; s < 6; ++s) x1[0][s] = pack8(xsA[s >> 1], s & 1);
#pragma unroll
    for (int n = 0; n < RD; ++n) ring[n] = wload(c, 72 + n);
    phaseS(IC<1>{});
  }
}

extern "C" void kernel_launch(void* const* d_in, const int* in_sizes, int n_in, void* d_out, int out_size,
                              void* d_ws, size_t ws_size, hipStream_t stream) {
  (void)hipFuncSetAttribute((const void*)swin_kernel, hipFuncAttributeMaxDynamicSharedMemorySize, LDS_TOTAL);
  swin_kernel<<<NWG, 512, LDS_TOTAL, stream>>>((const char*)d_in[0], (const char*)d_in[1], (const char*)d_in[2],
                                               (const char*)d_in[3], (const char*)d_in[4], (char*)d_out, (char*)d_ws);
}
```

```cpp
#include <hip/hip_runtime.h>
#include <hip/hip_bf16.h>
#include <stdint.h>

using bf16x8 = __attribute__((ext_vector_type(8))) short;
using f32x16 = __attribute__((ext_vector_type(16))) float;
using u32x4 = __attribute__((ext_vector_type(4))) unsigned;
using f32x2 = __attribute__((ext_vector_type(2))) float;
using bf2 = __attribute__((ext_vector_type(2))) __bf16;
#if defined(__HIP_DEVICE_COMPILE__)
#define NOPK __attribute__((target("no-packed-fp32-ops")))
#else
#define NOPK
#endif
#define DI __device__ __forceinline__ NOPK
#define MFMA(a, b, c) __builtin_amdgcn_mfma_f32_32x32x16_bf16(__builtin_bit_cast(bf16x8, (a)), __builtin_bit_cast(bf16x8, (b)), (c), 0, 0, 0)
#define SB() __builtin_amdgcn_sched_barrier(0)
#define PIN(x) asm volatile("" : "+v"(x))
#define STORE_AUX 16
#define CODEPF 32768

constexpr int CH = 96, HH = 224, WW = 224;
constexpr int ROWB = CH * 2;
constexpr int FRAMEB = HH * WW * ROWB;
constexpr int IMGROWB = WW * ROWB;
constexpr int NWIN = 28;
constexpr int NUNITS = NWIN * NWIN;
constexpr int WMAT_BYTES = 18 * 1024;
constexpr float LN_EPS = 1e-5f;
constexpr int NWG = 256;
constexpr float QK_SCALE = 0.10206207261596575f * 1.4426950408889634f;

template <int I> struct IC { static constexpr int v = I; };
template <int B, int E, class F> DI void sfor(F&& f) { if constexpr (B < E) { f(IC<B>{}); sfor<B + 1, E>(f); } }
#define LAM(M) [&](auto M) __attribute__((always_inline)) NOPK

DI unsigned pk2(float a, float b) {
  f32x2 v = {a, b};
  return __builtin_bit_cast(unsigned, __builtin_convertvector(v, bf2));
}
DI float lo16(unsigned u) { return __builtin_bit_cast(float, u << 16); }
DI float hi16(unsigned u) { return __builtin_bit_cast(float, u & 0xffff0000u); }
DI unsigned fbits(float f) { return __builtin_bit_cast(unsigned, f); }
DI float bitsf(unsigned u) { return __builtin_bit_cast(float, u); }
DI float xhalf_sum(float v) {
  auto r = __builtin_amdgcn_permlane32_swap(fbits(v), fbits(v), false, false);
  return bitsf(r[0]) + bitsf(r[1]);
}
DI float xhalf_max(float v) {
  auto r = __builtin_amdgcn_permlane32_swap(fbits(v), fbits(v), false, false);
  return __builtin_fmaxf(bitsf(r[0]), bitsf(r[1]));
}
DI float max3(float a, float b, float c) { return __builtin_fmaxf(__builtin_fmaxf(a, b), c); }
DI u32x4 pack8(const f32x16& a, int s) {
  u32x4 r;
  r[0] = pk2(a[8 * s + 0], a[8 * s + 1]);
  r[1] = pk2(a[8 * s + 2], a[8 * s + 3]);
  r[2] = pk2(a[8 * s + 4], a[8 * s + 5]);
  r[3] = pk2(a[8 * s + 6], a[8 * s + 7]);
  return r;
}

typedef __attribute__((address_space(3))) char lds_char;
typedef __attribute__((address_space(3))) unsigned lds_u32;
DI u32x4 lds_ld(const lds_char* p) { return *(const __attribute__((address_space(3))) u32x4*)p; }
DI void lds_st(lds_char* p, u32x4 v) { *(__attribute__((address_space(3))) u32x4*)p = v; }

constexpr int LDS_CNT = 0;
DI void cnt_arrive(const lds_char* l16, unsigned dvoff, int k) {
  asm volatile("s_lshl_b64 exec, 1, %2\n\tds_add_u32 %0, %1 offset:%3\n\ts_mov_b64 exec, -1"
               :: "v"((unsigned)(size_t)l16), "v"(dvoff), "s"(k), "i"(LDS_CNT) : "memory", "scc");
}
DI unsigned cnt_peek(const lds_char* l16) {
  return __hip_atomic_load((const lds_u32*)(l16 + LDS_CNT), __ATOMIC_RELAXED, __HIP_MEMORY_SCOPE_WORKGROUP);
}
DI void cnt_wait(const lds_char* l16, int k, unsigned n, unsigned peeked) {
  const unsigned target = n * 16u * (unsigned)k;
  asm volatile("" ::: "memory");
  if (__builtin_expect((unsigned)__builtin_amdgcn_readlane(peeked, k) < target, 0)) {
    for (;;) {
      __builtin_amdgcn_s_sleep(1);
      if ((unsigned)__builtin_amdgcn_readlane(cnt_peek(l16), k) >= target) break;
    }
  }
  asm volatile("" ::: "memory");
}
DI void cnt_wait(const lds_char* l16, int k, unsigned n) { cnt_wait(l16, k, n, cnt_peek(l16)); }

typedef __attribute__((address_space(1))) unsigned gu32;
constexpr int SCR_OFF = 65536;
constexpr int XFS = 1552;
constexpr int XREG = 6304;
constexpr int OSTG = 4096;
constexpr int LDS_ID = 256;
constexpr int LDS_W0 = LDS_ID + 2048;
constexpr int LDS_X = LDS_W0 + 2 * WMAT_BYTES;
constexpr int LDS_W2 = LDS_X + 8 * XREG;
constexpr int LDS_O = LDS_W2 + 2 * WMAT_BYTES;
constexpr int LDS_RAW = LDS_W2;
constexpr int LDS_RAW_END = LDS_RAW + 73728;
constexpr int LDS_TOTAL = LDS_RAW_END;
static_assert(LDS_O + 8 * OSTG <= LDS_RAW_END && LDS_O + 2 * WMAT_BYTES <= LDS_RAW_END, "staging");
static_assert(LDS_TOTAL <= 163840, "lds");

DI void dma_tile(const char* row, unsigned voff, unsigned lds) {
  const char* r1 = row + FRAMEB; const char* r2 = row + 2 * (size_t)FRAMEB; const char* r3 = row + 3 * (size_t)FRAMEB;
  unsigned keep;
  asm volatile(
      "s_mov_b32 %0, m0\n\t"
      "s_mov_b32 m0, %6\n\ts_nop 0\n\t"
      "global_load_lds_dwordx4 %1, %2 nt\n\t"
      "s_add_u32 m0, m0, %7\n\ts_nop 0\n\t"
      "global_load_lds_dwordx4 %1, %3 nt\n\t"
      "s_add_u32 m0, m0, %7\n\ts_nop 0\n\t"
      "global_load_lds_dwordx4 %1, %4 nt\n\t"
      "s_add_u32 m0, m0, %7\n\ts_nop 0\n\t"
      "global_load_lds_dwordx4 %1, %5 nt\n\t"
      "s_mov_b32 exec_hi, 0\n\t"
      "global_load_lds_dwordx4 %1, %5 offset:1024 nt\n\t"
      "s_sub_u32 m0, m0, %7\n\ts_nop 0\n\t"
      "global_load_lds_dwordx4 %1, %4 offset:1024 nt\n\t"
      "s_sub_u32 m0, m0, %7\n\ts_nop 0\n\t"
      "global_load_lds_dwordx4 %1, %3 offset:1024 nt\n\t"
      "s_sub_u32 m0, m0, %7\n\ts_nop 0\n\t"
      "global_load_lds_dwordx4 %1, %2 offset:1024 nt\n\t"
      "s_mov_b32 exec_hi, -1\n\t"
      "s_mov_b32 m0, %0"
      : "=&s"(keep) : "v"(voff), "s"(row), "s"(r1), "s"(r2), "s"(r3), "s"(lds), "i"(XFS) : "memory", "scc");
}
DI void dma_tile_sc1(const char* row, unsigned voff, unsigned lds) {
  const char* r1 = row + FRAMEB; const char* r2 = row + 2 * (size_t)FRAMEB; const char* r3 = row + 3 * (size_t)FRAMEB;
  unsigned keep;
  asm volatile(
      "s_mov_b32 %0, m0\n\t"
      "s_mov_b32 m0, %6\n\ts_nop 0\n\t"
      "global_load_lds_dwordx4 %1, %2 sc1\n\t"
      "s_add_u32 m0, m0, %7\n\ts_nop 0\n\t"
      "global_load_lds_dwordx4 %1, %3 sc1\n\t"
      "s_add_u32 m0, m0, %7\n\ts_nop 0\n\t"
      "global_load_lds_dwordx4 %1, %4 sc1\n\t"
      "s_add_u32 m0, m0, %7\n\ts_nop 0\n\t"
      "global_load_lds_dwordx4 %1, %5 sc1\n\t"
      "s_mov_b32 exec_hi, 0\n\t"
      "global_load_lds_dwordx4 %1, %5 offset:1024 sc1\n\t"
      "s_sub_u32 m0, m0, %7\n\ts_nop 0\n\t"
      "global_load_lds_dwordx4 %1, %4 offset:1024 sc1\n\t"
      "s_sub_u32 m0, m0, %7\n\ts_nop 0\n\t"
      "global_load_lds_dwordx4 %1, %3 offset:1024 sc1\n\t"
      "s_sub_u32 m0, m0, %7\n\ts_nop 0\n\t"
      "global_load_lds_dwordx4 %1, %2 offset:1024 sc1\n\t"
      "s_mov_b32 exec_hi, -1\n\t"
      "s_mov_b32 m0, %0"
      : "=&s"(keep) : "v"(voff), "s"(row), "s"(r1), "s"(r2), "s"(r3), "s"(lds), "i"(XFS) : "memory", "scc");
}
DI void dma_piece(const char* base, unsigned voff, unsigned lds) {
  unsigned keep;
  asm volatile("s_mov_b32 %0, m0\n\ts_mov_b32 m0, %3\n\ts_nop 0\n\tglobal_load_lds_dwordx4 %1, %2\n\ts_mov_b32 m0, %0"
               : "=&s"(keep) : "v"(voff), "s"(base), "s"(lds) : "memory");
}
#define VMWAIT(n) asm volatile("s_waitcnt vmcnt(" #n ")" ::: "memory")
#define LGWAIT() asm volatile("s_waitcnt lgkmcnt(0)" ::: "memory")

template <int CPR, int SS>
DI void raw_piece(const char* src, int piece, int lane, unsigned lds_img) {
  const unsigned q = (unsigned)(piece * 64 + lane);
  const unsigned row = (q * (unsigned)(((1 << 20) + CPR - 1) / CPR)) >> 20;
  const unsigned off = (SS == CPR * 16) ? 16u * (q ^ ((row >> 2) & 3u)) : row * (unsigned)(SS - CPR * 16) + 16u * (q ^ ((row >> 2) & 3u));
  dma_piece(src, off, lds_img + piece * 1024);
}
typedef short s16x4 __attribute__((ext_vector_type(4)));
DI void fold_read_rows(u32x4* d, const lds_char* img, int rb, int row0, int sw, int hh) {
  const int x = (sw >> 2) & 3;
  const lds_char* b0 = img + (row0 + sw) * rb + 16 * (hh ^ x);
  const lds_char* b1 = img + (row0 + sw) * rb + 16 * ((2 + hh) ^ x);
#pragma unroll
  for (int s = 0; s < 6; ++s) d[s] = lds_ld(((s & 1) ? b1 : b0) + 64 * (s >> 1));
}
DI void fold_read_cols(u32x4* d, const lds_char* img, int T, int lane) {
  const int hh = lane >> 5, cb = (lane >> 4) & 1, qq = (lane >> 2) & 3, pp = lane & 3;
  const int cc = 4 * T + 2 * cb + (pp & 1);
  const lds_char* b0 = img + (8 * hh + qq) * 192 + 16 * (cc ^ ((2 * hh) & 3)) + 8 * (pp >> 1);
  const lds_char* b1 = img + (8 * hh + qq + 4) * 192 + 16 * (cc ^ ((2 * hh + 1) & 3)) + 8 * (pp >> 1);
#pragma unroll
  for (int s = 0; s < 6; ++s) {
    s16x4 lo = __builtin_amdgcn_ds_read_tr16_b64_v4i16((__attribute__((address_space(3))) s16x4*)(b0 + s * 16 * 192));
    s16x4 hi = __builtin_amdgcn_ds_read_tr16_b64_v4i16((__attribute__((address_space(3))) s16x4*)(b1 + s * 16 * 192));
    d[s] = __builtin_bit_cast(u32x4, __builtin_shufflevector(lo, hi, 0, 1, 2, 3, 4, 5, 6, 7));
  }
}
DI void fold_compute(const u32x4 (&a)[18], const u32x4 (&b)[6], float sc, u32x4 (&res)[6]) {
  const f32x16 zero = {};
  f32x16 acc[3];
#pragma unroll
  for (int s = 0; s < 6; ++s)
#pragma unroll
    for (int Tin = 0; Tin < 3; ++Tin) acc[Tin] = MFMA(a[Tin * 6 + s], b[s], s == 0 ? zero : acc[Tin]);
#pragma unroll
  for (int Tin = 0; Tin < 3; ++Tin) {
#pragma unroll
    for (int i = 0; i < 16; ++i) acc[Tin][i] *= sc;
    res[2 * Tin] = pack8(acc[Tin], 0);
    res[2 * Tin + 1] = pack8(acc[Tin], 1);
  }
}
DI void fold_store(lds_char* wdst, const u32x4 (&res)[6]) {
#pragma unroll
  for (int s = 0; s < 6; ++s) lds_st(wdst + s * 1024, res[s]);
}

struct Stats { float s0, s1, q0, q1, rs, nb; };
DI void stat_pkt(const u32x4 (&xf)[6], f32x16 (&xs)[3], Stats& st, int k) {
  const int s = k >> 2, i = k & 3, T = s >> 1, e = 8 * (s & 1) + 2 * i;
  const float a = lo16(xf[s][i]), b = hi16(xf[s][i]);
  xs[T][e] = a; xs[T][e + 1] = b;
  if (k == 0) { st.s0 = a; st.s1 = b; st.q0 = a * a; st.q1 = b * b; }
  else { st.s0 += a; st.s1 += b; st.q0 = __builtin_fmaf(a, a, st.q0); st.q1 = __builtin_fmaf(b, b, st.q1); }
  PIN(st.s0); PIN(st.s1); PIN(st.q0); PIN(st.q1);
}
DI void stat_fin(Stats& st) {
  float s = xhalf_sum(st.s0 + st.s1), q = xhalf_sum(st.q0 + st.q1);
  float m = s * (1.f / CH);
  float var = __builtin_fmaxf(__builtin_fmaf(-m, m, q * (1.f / CH)), 0.f);
  st.rs = __builtin_amdgcn_rsqf(var + LN_EPS);
  st.nb = -m * st.rs;
  PIN(st.rs); PIN(st.nb);
}
DI void nrm_pkt(const f32x16 (&xs)[3], u32x4 (&hf)[6], const Stats& st, int k) {
  const int s = k >> 2, i = k & 3, T = s >> 1, e = 8 * (s & 1) + 2 * i;
  unsigned v = pk2(__builtin_fmaf(xs[T][e], st.rs, st.nb), __builtin_fmaf(xs[T][e + 1], st.rs, st.nb));
  PIN(v);
  hf[s][i] = v;
}
DI void cvt4(u32x4& d, const f32x16& a, int s) { d = pack8(a, s); PIN(d); }

constexpr int RD = 4;
constexpr int NWSEQ = 144;
DI constexpr int woff(int n) {
  n %= NWSEQ;
  if (n < 72) {
    int k = n % 36;
    if (k < 12) return LDS_W0 + (k & 1) * WMAT_BYTES + (k >> 1) * 1024;
    if (k < 24) return LDS_W0 + (6 + (k - 12)) * 1024;
    return LDS_W0 + WMAT_BYTES + (6 + (k - 24)) * 1024;
  }
  if (n < 108) return LDS_W2 + ((n - 72) % 18) * 1024;
  return LDS_W2 + WMAT_BYTES + (((n - 108) / 12) * 6 + (n - 108) % 6) * 1024;
}
constexpr int WB_BASE = LDS_W2;
struct Ctx {
  const lds_char* wa;
  const lds_char* wb;
  bool ms0, ms1, mg[4];
};
DI u32x4 wload(const Ctx& c, int n) {
  const int o = woff(n);
  return o < LDS_W2 ? lds_ld(c.wa + o) : lds_ld(c.wb + (o - WB_BASE));
}
#define WUSE(n) ring[(n) % RD]
#define WNEXT(n) ring[(n) % RD] = wload(c, (n) + RD)

struct Slot { int ty, T, s; };
DI constexpr Slot tslot(int m) {
  if (m < 12) return {m & 1, 0, m >> 1};
  if (m < 15) return {0, 1, m - 12};
  if (m < 17) return {2, 0, m - 15};
  if (m < 20) return {0, 1, m - 14};
  if (m < 23) return {0, 2, m - 20};
  if (m < 25) return {2, 1, m - 23};
  if (m < 28) return {0, 2, m - 22};
  if (m < 31) return {1, 1, m - 28};
  if (m < 33) return {2, 2, m - 31};
  if (m < 36) return {1, 1, m - 30};
  if (m < 42) return {1, 2, m - 36};
  return {3, (m - 42) >> 1, (m - 42) & 1};
}
DI constexpr int twidx(int m) {
  return m < 15 ? m : (m < 17 ? 15 : (m < 23 ? m - 2 : (m < 25 ? 21 : (m < 31 ? m - 4 : (m < 33 ? 27 : (m < 42 ? m - 6 : 36))))));
}
DI constexpr int sched_cnt(int m, int first, int last, int total) {
  return m <= first ? 0 : (m >= last ? total : (m - first) * total / (last - first));
}

struct TState { f32x16 g[3], v[3], sa; u32x4 hf[6], gp[2], vp[3][2], pf[2]; float e[4]; unsigned pk0, pk1; };
template <int K0, int K1>
DI void stat_pkts(const u32x4 (&xf)[6], f32x16 (&xs)[3], Stats& st) {
  if constexpr (K0 < K1) { stat_pkt(xf, xs, st, K0); stat_pkts<K0 + 1, K1>(xf, xs, st); }
}
template <int N0, int m, int M1, int HS, bool PE, bool OE>
DI void temporal_slots(const Ctx& c, u32x4 (&ring)[RD], TState& t, f32x16 (&xs)[3], const Stats& st,
                       const u32x4 (&nx)[6], f32x16 (&nxs)[3], Stats& nst, u32x4 (&nhf)[6], const f32x16 (&pxs)[3], lds_char* xw) {
  const f32x16 zero = {};
  if constexpr (m < M1) {
    constexpr Slot sl = tslot(m);
    constexpr int n = N0 + twidx(m);
    if constexpr (sl.ty == 0) { t.g[sl.T] = MFMA(WUSE(n), t.hf[sl.s], sl.s == 0 ? zero : t.g[sl.T]); WNEXT(n); }
    else if constexpr (sl.ty == 1) { t.v[sl.T] = MFMA(t.hf[sl.s], WUSE(n), sl.s == 0 ? zero : t.v[sl.T]); WNEXT(n); }
    else if constexpr (sl.ty == 2) t.sa = MFMA(t.hf[2 * sl.T + sl.s], t.gp[sl.s], (sl.T == 0 && sl.s == 0) ? zero : t.sa);
    else xs[sl.T] = MFMA(t.vp[sl.T][sl.s], t.pf[sl.s], xs[sl.T]);
    SB();
    if constexpr (m < 10) { nrm_pkt(xs, t.hf, st, 4 + 2 * m); nrm_pkt(xs, t.hf, st, 5 + 2 * m); }
    if constexpr (PE && m >= 2 && m < 8) { u32x4 a; cvt4(a, pxs[(m - 2) >> 1], m & 1); lds_st(xw + (m - 2) * 1024, a); }
    if constexpr (m == 12) cvt4(t.gp[0], t.g[0], 0);
    if constexpr (m == 13) cvt4(t.gp[1], t.g[0], 1);
    if constexpr (m == 14) cvt4(t.vp[0][0], t.v[0], 0);
    if constexpr (m == 15) cvt4(t.vp[0][1], t.v[0], 1);
    if constexpr (m == 21) { cvt4(t.gp[0], t.g[1], 0); }
    if constexpr (m == 22) { cvt4(t.gp[1], t.g[1], 1); }
    if constexpr (m == 29) { cvt4(t.gp[0], t.g[2], 0); }
    if constexpr (m == 30) { cvt4(t.gp[1], t.g[2], 1); }
    if constexpr (m == 35 || m == 36) {
#pragma unroll
      for (int k = 2 * (m - 35); k < 2 * (m - 35) + 2; ++k) {
        float s1 = t.sa[4 + k], s3 = t.sa[12 + k];
        asm("" : "+v"(s1)); asm("" : "+v"(s3));
        float a = c.ms0 ? s1 : t.sa[k];
        float b = c.ms0 ? s3 : t.sa[8 + k];
        t.e[k] = c.ms1 ? b : a; PIN(t.e[k]);
      }
    }
    if constexpr (m == 37) {
      float mx = max3(max3(t.e[0], t.e[1], t.e[2]), t.e[3], t.e[3]);
#pragma unroll
      for (int k = 0; k < 4; ++k) { t.e[k] = __builtin_amdgcn_exp2f(t.e[k] - mx); PIN(t.e[k]); }
    }
    if constexpr (m == 38) {
      float rinv = __builtin_amdgcn_rcpf((t.e[0] + t.e[1]) + (t.e[2] + t.e[3]));
      t.pk0 = pk2(t.e[0] * rinv, t.e[1] * rinv); t.pk1 = pk2(t.e[2] * rinv, t.e[3] * rinv);
      PIN(t.pk0); PIN(t.pk1);
    }
    if constexpr (m == 39) {
#pragma unroll
      for (int g = 0; g < 4; ++g) {
        t.pf[g >> 1][(g & 1) * 2 + 0] = c.mg[g] ? t.pk0 : 0u;
        t.pf[g >> 1][(g & 1) * 2 + 1] = c.mg[g] ? t.pk1 : 0u;
      }
      PIN(t.pf[0]); PIN(t.pf[1]);
    }
    if constexpr (m == 40) { cvt4(t.vp[1][0], t.v[1], 0); cvt4(t.vp[1][1], t.v[1], 1); }
    if constexpr (m == 44) { cvt4(t.vp[2][0], t.v[2], 0); cvt4(t.vp[2][1], t.v[2], 1); }
    if constexpr (m < 35) stat_pkts<sched_cnt(m, HS + 2, 35, 20), sched_cnt(m + 1, HS + 2, 35, 20)>(nx, nxs, nst);
    if constexpr (m == 41) stat_pkts<20, 22>(nx, nxs, nst);
    if constexpr (m == 42) stat_pkts<22, 24>(nx, nxs, nst);
    if constexpr (m == 43) stat_fin(nst);
    if constexpr (m == 45) { nrm_pkt(nxs, nhf, nst, 0); nrm_pkt(nxs, nhf, nst, 1); }
    if constexpr (m == 46) { nrm_pkt(nxs, nhf, nst, 2); nrm_pkt(nxs, nhf, nst, 3); }
    if constexpr (OE && m == 46) { u32x4 a; cvt4(a, xs[0], 0); lds_st(xw, a); }
    if constexpr (OE && m == 47) { u32x4 a; cvt4(a, xs[0], 1); lds_st(xw + 1024, a); }
    SB();
    temporal_slots<N0, m + 1, M1, HS, PE, OE>(c, ring, t, xs, st, nx, nxs, nst, nhf, pxs, xw);
  }
}

struct SMState { float m0, m1, mx, l0, l1, rinv; };
template <int u>
DI void sm_unit(f32x16& a, f32x16& b, SMState& s, u32x4 (&pf)[4]) {
  if constexpr (u < 4) {
    f32x16& x = (u < 2) ? a : b;
    const int o = 8 * (u & 1);
    if (u == 0) { s.m0 = max3(x[0], x[1], x[2]); s.m1 = max3(x[3], x[4], x[5]); s.m0 = max3(s.m0, x[6], x[7]); }
    else { s.m0 = max3(s.m0, x[o], x[o + 1]); s.m1 = max3(s.m1, x[o + 2], x[o + 3]); s.m0 = max3(s.m0, x[o + 4], x[o + 5]); s.m1 = max3(s.m1, x[o + 6], x[o + 7]); }
    PIN(s.m0); PIN(s.m1);
  } else if constexpr (u == 4) {
    s.mx = xhalf_max(__builtin_fmaxf(s.m0, s.m1));
    if (__builtin_expect(__builtin_amdgcn_ballot_w64(!(__builtin_fabsf(s.mx) <= 40.f)) != 0ull, 0)) {
#pragma unroll
      for (int i = 0; i < 16; ++i) { a[i] -= s.mx; b[i] -= s.mx; }
    }
  } else if constexpr (u < 21) {
    constexpr int k = u - 5;
    f32x16& x = (k < 8) ? a : b;
    constexpr int o = 2 * (k & 7);
    float e0 = __builtin_amdgcn_exp2f(x[o]), e1 = __builtin_amdgcn_exp2f(x[o + 1]);
    PIN(e0); PIN(e1);
    x[o] = e0; x[o + 1] = e1;
    if constexpr (k >= 2) {
      f32x16& y = (k - 1 < 8) ? a : b;
      constexpr int o1 = 2 * ((k - 1) & 7);
      if constexpr (k == 2) { s.l0 = a[0] + y[o1]; s.l1 = a[1] + y[o1 + 1]; } else { s.l0 += y[o1]; s.l1 += y[o1 + 1]; }
      PIN(s.l0); PIN(s.l1);
    }
  } else if constexpr (u == 21) {
    s.rinv = __builtin_amdgcn_rcpf(xhalf_sum((s.l0 + b[14]) + (s.l1 + b[15]))); PIN(s.rinv);
  } else {
    const int k = u - 22;
    f32x16& x = (k < 4) ? a : b;
    const int o = 4 * (k & 3);
    unsigned p0 = pk2(x[o] * s.rinv, x[o + 1] * s.rinv), p1 = pk2(x[o + 2] * s.rinv, x[o + 3] * s.rinv);
    PIN(p0); PIN(p1);
    pf[k >> 1][2 * (k & 1)] = p0; pf[k >> 1][2 * (k & 1) + 1] = p1;
  }
}

__global__ __launch_bounds__(512, 2) NOPK void swin_kernel(const char* __restrict__ x, const char* __restrict__ wqkv_t,
                                                           const char* __restrict__ wo_t, const char* __restrict__ wqkv_s,
                                                           const char* __restrict__ wo_s, char* __restrict__ out,
                                                           char* __restrict__ ws) {
  extern __shared__ __attribute__((aligned(16))) char smem_[];
  lds_char* smem = (lds_char*)smem_;
  const unsigned smem_a = (unsigned)(size_t)smem;
  const int tid = threadIdx.x, lane = tid & 63;
  const int wid8 = __builtin_amdgcn_readfirstlane(tid >> 6);
  const int grp = wid8 >> 2, wid = wid8 & 3;
  const int r = lane & 31, hh = lane >> 5;
  const int p = r >> 2, f = r & 3;
  const int sw = (r & 0x13) | ((r & 4) << 1) | ((r & 8) >> 1);
  const unsigned dvoff = lane * 16;
  const unsigned xr_a = smem_a + LDS_X + wid8 * XREG;

  constexpr int FULL = 0, SONLY = 2, NONE = 3;
  const int bid = blockIdx.x;
  const int xj = bid >> 3, ub = (bid & 7) * 98;
  const bool shelp = xj == 8 || xj == 9, thelp = xj < 8 && grp == 0;
  const int li = shelp ? 2 * (xj - 8) + grp : (xj >> 1) & 3, ti = xj & 1;
  const int lunit = ub + 94 + li, hslot = ((bid & 7) * 4 + li) * 4 + wid;
  const int jj = xj < 8 ? xj : xj - 2;
  int unit = shelp ? ub + 90 + li : ub + jj + 30 * grp;
  int nmode = shelp ? SONLY : (grp == 0 ? FULL : NONE), nunit = shelp ? lunit : ub + jj + 60;
  const lds_char* l16 = smem + lane * 16;
  constexpr int C_TW = 5, C_MS = 6, C_RAW = 7, C_RDT = 8;
  constexpr int IMG = WMAT_BYTES;
  gu32* hfl = (gu32*)(ws + hslot * 256);
  unsigned hev, cpf = 0, wpf = 0, hepoch;
  {
    {
      unsigned long long pc; asm volatile("s_getpc_b64 %0" : "=s"(pc));
      const char* cb = (const char*)(pc & ~255ull);
      const unsigned o = (unsigned)(((xj & 7) * 8 + wid8) * (CODEPF / 64) + (lane * 64 < CODEPF / 64 ? lane * 64 : 0));
      asm volatile("global_load_dword %0, %1, %2" : "+v"(cpf) : "v"(o), "s"(cb) : "memory");
    }
    asm volatile("global_load_dword %0, %1, %2 sc1" : "=v"(hev) : "v"(0u), "s"(hfl) : "memory");
    {
      const int line = ((xj & 7) * 8 + wid8) * 9 + (lane < 9 ? lane : 0);
      const char* a = line < 432 ? wqkv_s + 128 * line : wo_s + 128 * (line - 432);
      asm volatile("global_load_dword %0, %1, off" : "+v"(wpf) : "v"(a) : "memory");
    }
    {
      int pc = (wid8 * 7 + xj * 5) % 54;
#pragma nounroll
      for (int i = 0; i < 7; ++i) { raw_piece<36, 576>(wqkv_t, pc, lane, smem_a + LDS_RAW); pc = pc == 53 ? 0 : pc + 1; }
      pc = (wid8 * 3 + xj) % 18;
#pragma nounroll
      for (int i = 0; i < 3; ++i) { raw_piece<12, 192>(wo_t, pc, lane, smem_a + LDS_RAW + 3 * IMG); pc = pc == 17 ? 0 : pc + 1; }
    }
    if (tid < 64) ((lds_u32*)(smem + LDS_CNT))[tid] = 0u;
    const int u0 = thelp ? lunit : unit, wy0 = u0 / NWIN, wx0 = u0 % NWIN;
    const char* x0 = x + (size_t)(wy0 * 8 + 2 * wid + (thelp ? ti : 0)) * IMGROWB + (size_t)(wx0 * 8) * ROWB;
    asm volatile("s_waitcnt vmcnt(0) lgkmcnt(0)\n\ts_barrier" : "+v"(hev), "+v"(cpf), "+v"(wpf) :: "memory");
    hepoch = __builtin_amdgcn_readfirstlane(hev);
    if (grp == 0) dma_tile(x0, dvoff, xr_a);
    u32x4 fa[18], fb[6], res[6];
    auto job_read = [&](int j) __attribute__((always_inline)) NOPK {
      const lds_char* aimg = smem + LDS_RAW + (j < 3 ? 384 : 0);
#pragma unroll
      for (int Tin = 0; Tin < 3; ++Tin) fold_read_rows(fa + 6 * Tin, aimg, 576, 32 * Tin, sw, hh);
      if (j < 3) fold_read_cols(fb, smem + LDS_RAW + 3 * IMG, j, lane);
      else fold_read_rows(fb, smem + LDS_RAW + 192, 576, 32 * (j - 3), sw, hh);
    };
    if (grp == 1) {
      __builtin_amdgcn_s_setprio(1);
      const int ja = wid < 2 ? wid : 2 * wid - 2;
#pragma nounroll
      for (int stage = 0; stage < 2; ++stage) {
        lds_char* wdst = smem + (stage ? LDS_W2 : LDS_W0) + lane * 16;
        u32x4 res2[6];
        job_read(ja);
        fold_compute(fa, fb, ja < 3 ? 1.f : QK_SCALE, res);
        if (stage == 0) fold_store(wdst + (ja < 3 ? IMG : 0) + (ja % 3) * 6144, res);
        if (wid >= 2) {
          job_read(ja + 1);
          cnt_arrive(l16, dvoff, C_RDT);
          fold_compute(fa, fb, QK_SCALE, res2);
        } else cnt_arrive(l16, dvoff, C_RDT);
        if (stage == 0) {
          if (wid >= 2) fold_store(wdst + ((ja + 1) % 3) * 6144, res2);
          cnt_arrive(l16, dvoff, C_TW);
          cnt_wait(l16, C_RDT, 4);
          int pc = (wid * 14 + xj * 5) % 54;
#pragma nounroll
          for (int i = 0; i < 14; ++i) { raw_piece<36, 576>(wqkv_s, pc, lane, smem_a + LDS_RAW); pc = pc == 53 ? 0 : pc + 1; }
          pc = (wid * 5 + xj) % 18;
#pragma nounroll
          for (int i = 0; i < 5; ++i) { raw_piece<12, 192>(wo_s, pc, lane, smem_a + LDS_RAW + 3 * IMG); pc = pc == 17 ? 0 : pc + 1; }
          dma_tile(x0, dvoff, xr_a);
          VMWAIT(8);
          cnt_arrive(l16, dvoff, C_RAW);
          cnt_wait(l16, C_RAW, 4);
        } else {
          cnt_wait(l16, C_RDT, 8);
          fold_store(wdst + (ja < 3 ? IMG : 0) + (ja % 3) * 6144, res);
          if (wid >= 2) fold_store(wdst + ((ja + 1) % 3) * 6144, res2);
          cnt_arrive(l16, dvoff, C_MS);
        }
      }
      __builtin_amdgcn_s_setprio(0);
    } else if (wid8 == 3) {
      u32x4 idf[2];
#pragma unroll
      for (int s = 0; s < 2; ++s)
#pragma unroll
        for (int d = 0; d < 4; ++d)
          idf[s][d] = (s == (sw >> 4) && hh == ((sw >> 3) & 1) && d == ((sw & 7) >> 1)) ? (0x3F80u << (16 * (sw & 1))) : 0u;
      lds_st(smem + LDS_ID + lane * 16, idf[0]); lds_st(smem + LDS_ID + 1024 + lane * 16, idf[1]);
      cnt_arrive(l16, dvoff, C_MS);
    }
  }

  const int cW = 1 + 2 * grp, cR = 2 + 2 * grp;
  auto laddr = [&](unsigned off) __attribute__((always_inline)) NOPK { off += smem_a; asm("" : "+v"(off)); return (lds_char*)(size_t)off; };
  const unsigned xg = LDS_X + grp * 4 * XREG;
  lds_char* xw = laddr(xg + f * XREG + (hh * 32 + 8 * wid + p) * 16);
  const lds_char* xr = laddr(xg + wid * XREG + lane * 16);
  const lds_char* xl = laddr(xg + wid * XREG + f * XFS + p * ROWB + hh * 16);
  lds_char* ow0 = laddr(LDS_O + wid8 * OSTG + r * 64 + 16 * (hh ^ ((r >> 1) & 3)));
  lds_char* ow1 = laddr(LDS_O + wid8 * OSTG + r * 64 + 16 * ((2 + hh) ^ ((r >> 1) & 3)));
  const lds_char* ord = laddr(LDS_O + wid8 * OSTG + lane * 16);
  const f32x16 zero = {};
  Ctx c;
  c.wa = l16; c.wb = laddr(WB_BASE + lane * 16);
  {
    c.ms0 = (p >> 1) & 1;
    c.ms1 = (p >> 2) & 1;
    const bool valid = (hh == (p & 1));
#pragma unroll
    for (int g = 0; g < 4; ++g) c.mg[g] = valid && (p >> 1) == g;
  }
  unsigned ooff = (lane >> 5) * 2 * IMGROWB + ((lane >> 2) & 7) * ROWB + 16 * ((lane & 3) ^ ((lane >> 3) & 3));
  asm("" : "+v"(ooff));
  auto orsrc = __builtin_amdgcn_make_buffer_rsrc((void*)out, 0, 4 * FRAMEB, 0x00020000);

  f32x16 xsA[3], xsB[3], xsC[3]; Stats stA, stB, stC; u32x4 hfA[6];
  u32x4 x1[2][6], h2[2][6];
  u32x4 ring[RD];
  bool first = true, cold = true;
  unsigned ph = 0, pk;
  const char* nxu; int nstep; bool more;

  auto cold_start2 = [&]() __attribute__((always_inline)) NOPK {
    const int wy = unit / NWIN, wx = unit % NWIN;
    u32x4 xin[6];
#pragma unroll
    for (int s = 0; s < 6; ++s) xin[s] = lds_ld(xl + 32 * s);
    LGWAIT();
    dma_tile(x + (size_t)(wy * 8 + 2 * wid + (thelp ? 0 : 1)) * IMGROWB + (size_t)(wx * 8) * ROWB, dvoff, xr_a);
    stat_pkts<0, 24>(xin, xsA, stA);
    stat_fin(stA);
#pragma unroll
    for (int k = 0; k < 4; ++k) nrm_pkt(xsA, hfA, stA, k);
    cnt_wait(l16, C_TW, 4);
#pragma unroll
    for (int n = 0; n < RD; ++n) ring[n] = wload(c, n);
  };
  auto next_setup = [&]() __attribute__((always_inline)) NOPK {
    more = nmode != NONE;
    if (nmode == SONLY) { nxu = ws + SCR_OFF + (size_t)(hslot * 2) * 1536; nstep = 1536; }
    else { const int ny = nunit / NWIN, nx = nunit % NWIN; nxu = x + (size_t)(ny * 8 + 2 * wid) * IMGROWB + (size_t)(nx * 8) * ROWB; nstep = IMGROWB; }
  };
  auto phaseT = [&]() __attribute__((always_inline)) NOPK {
    TState t2;
    {
      TState t; u32x4 xin[6];
      t.hf[0] = hfA[0];
      temporal_slots<0, 0, 11, 10, false, false>(c, ring, t, xsA, stA, xin, xsB, stB, t2.hf, xsA, xw);
      if (cold) { VMWAIT(0); } else { VMWAIT(10); }
      if (cold && thelp && lane == 0)
        __hip_atomic_store(hfl + 32 + 16 * ti, hepoch + 1u, __ATOMIC_RELAXED, __HIP_MEMORY_SCOPE_AGENT);
#pragma unroll
      for (int s = 0; s < 6; ++s) xin[s] = lds_ld(xl + 32 * s);
      cnt_arrive(l16, dvoff, cR);
      temporal_slots<0, 11, 46, 10, false, false>(c, ring, t, xsA, stA, xin, xsB, stB, t2.hf, xsA, xw);
      pk = cnt_peek(l16);
      temporal_slots<0, 46, 48, 10, false, false>(c, ring, t, xsA, stA, xin, xsB, stB, t2.hf, xsA, xw);
    }
    {
      temporal_slots<36, 0, 2, 19, true, true>(c, ring, t2, xsB, stB, x1[0], xsC, stC, h2[0], xsA, xw);
      cnt_wait(l16, cR, ph + 4, pk);
      temporal_slots<36, 2, 8, 19, true, true>(c, ring, t2, xsB, stB, x1[0], xsC, stC, h2[0], xsA, xw);
      cnt_arrive(l16, dvoff, cW);
      temporal_slots<36, 8, 17, 19, true, true>(c, ring, t2, xsB, stB, x1[0], xsC, stC, h2[0], xsA, xw);
      pk = cnt_peek(l16);
      temporal_slots<36, 17, 20, 19, true, true>(c, ring, t2, xsB, stB, x1[0], xsC, stC, h2[0], xsA, xw);
      cnt_wait(l16, cW, ph + 4, pk);
#pragma unroll
      for (int s = 0; s < 6; ++s) x1[0][s] = lds_ld(xr + s * 1024);
      cnt_arrive(l16, dvoff, cR);
      if (first) cnt_wait(l16, C_MS, 5);
      temporal_slots<36, 20, 43, 19, true, true>(c, ring, t2, xsB, stB, x1[0], xsC, stC, h2[0], xsA, xw);
      pk = cnt_peek(l16);
      temporal_slots<36, 43, 46, 19, true, true>(c, ring, t2, xsB, stB, x1[0], xsC, stC, h2[0], xsA, xw);
      cnt_wait(l16, cR, ph + 8, pk);
      temporal_slots<36, 46, 48, 19, true, true>(c, ring, t2, xsB, stB, x1[0], xsC, stC, h2[0], xsA, xw);
    }
  };
  auto phaseS = [&](auto SOC) __attribute__((always_inline)) NOPK {
    constexpr bool SO = SOC.v != 0;
    const int wy = unit / NWIN, wx = unit % NWIN;
    f32x16 sa[2][2]; u32x4 gq[6];
    {
      f32x16 g[3], xsD[3]; Stats stD;
      sfor<0, 10>(LAM(M) {
        constexpr int m = M.v, n = 72 + m, T = m / 6, s = m % 6;
        g[T] = MFMA(WUSE(n), h2[0][s], s == 0 ? zero : g[T]); WNEXT(n);
        SB();
        if constexpr (m < 2) {
          if constexpr (!SO) {
            u32x4 a, b; cvt4(a, xsB[1 + m], 0); lds_st(xw + (2 + 2 * m) * 1024, a); cvt4(b, xsB[1 + m], 1); lds_st(xw + (3 + 2 * m) * 1024, b);
            if constexpr (m == 1) cnt_arrive(l16, dvoff, cW);
          }
        }
        if constexpr (m < 5) {
#pragma unroll
          for (int k = 4 + 4 * m; k < 8 + 4 * m; ++k) nrm_pkt(xsC, h2[0], stC, k);
        }
        if constexpr (m == 7) { cvt4(gq[0], g[0], 0); cvt4(gq[1], g[0], 1); }
        if constexpr (!SO && m == 8) pk = cnt_peek(l16);
        SB();
      });
      if constexpr (!SO) {
        cnt_wait(l16, cW, ph + 8, pk);
#pragma unroll
        for (int s = 0; s < 6; ++s) x1[1][s] = lds_ld(xr + s * 1024);
      } else {
        VMWAIT(10);
#pragma unroll
        for (int s = 0; s < 6; ++s) x1[1][s] = lds_ld(xl + 32 * s);
      }
      sfor<10, 24>(LAM(M) {
        constexpr int m = M.v;
        if constexpr (m < 12) { constexpr int n = 72 + m; g[1] = MFMA(WUSE(n), h2[0][m - 6], g[1]); WNEXT(n); }
        else if constexpr (m < 14) sa[0][0] = MFMA(h2[0][m - 12], gq[m - 12], m == 12 ? zero : sa[0][0]);
        else if constexpr (m < 20) { constexpr int n = 72 + m - 2; g[2] = MFMA(WUSE(n), h2[0][m - 14], m == 14 ? zero : g[2]); WNEXT(n); }
        else sa[0][0] = MFMA(h2[0][m - 18], gq[m - 18], sa[0][0]);
        SB();
        if constexpr (m == 13) {
          LGWAIT();
          if (!SO && nmode == FULL) dma_tile(nxu, dvoff, xr_a);
        }
        if constexpr (m == 14) { cvt4(gq[2], g[1], 0); cvt4(gq[3], g[1], 1); }
        if constexpr (m == 21) { cvt4(gq[4], g[2], 0); cvt4(gq[5], g[2], 1); }
        stat_pkts<sched_cnt(m, 11, 20, 24), sched_cnt(m + 1, 11, 20, 24)>(x1[1], xsD, stD);
        if constexpr (m == 19) stat_fin(stD);
        if constexpr (m >= 20) {
#pragma unroll
          for (int k = 6 * (m - 20); k < 6 * (m - 19); ++k) nrm_pkt(xsD, h2[1], stD, k);
        }
        SB();
      });
    }
    u32x4 pf[2][4];
    {
      f32x16 g[3]; u32x4 gp[2][2]; SMState sm;
      sfor<0, 36>(LAM(M) {
        constexpr int m = M.v;
        if constexpr (m < 6) sa[0][1] = MFMA(h2[1][m], gq[m], m == 0 ? zero : sa[0][1]);
        else if constexpr (m < 18) { constexpr int n = 90 + m - 6, T = (m - 6) / 6, s = (m - 6) % 6; g[T] = MFMA(WUSE(n), h2[1][s], s == 0 ? zero : g[T]); WNEXT(n); }
        else if constexpr (m < 22) { constexpr int kt = (m - 18) & 1, s = (m - 18) >> 1; sa[1][kt] = MFMA(h2[kt][s], gp[0][s], s == 0 ? zero : sa[1][kt]); }
        else if constexpr (m < 28) { constexpr int n = 90 + m - 10, s = m - 22; g[2] = MFMA(WUSE(n), h2[1][s], s == 0 ? zero : g[2]); WNEXT(n); }
        else { constexpr int T = (m - 28) / 4 + 1, kt = (m - 28) & 1, s = ((m - 28) >> 1) & 1; sa[1][kt] = MFMA(h2[kt][2 * T + s], gp[T & 1][s], sa[1][kt]); }
        SB();
        if constexpr (m == 14) { cvt4(gp[0][0], g[0], 0); cvt4(gp[0][1], g[0], 1); }
        if constexpr (m == 20) { cvt4(gp[1][0], g[1], 0); cvt4(gp[1][1], g[1], 1); }
        if constexpr (m == 30) { cvt4(gp[0][0], g[2], 0); cvt4(gp[0][1], g[2], 1); }
        if constexpr (m == 2 || m == 3) sm_unit<m - 2>(sa[0][0], sa[0][1], sm, pf[0]);
        if constexpr (m >= 8) sm_unit<m - 6>(sa[0][0], sa[0][1], sm, pf[0]);
        SB();
      });
    }
    if (!SO && nmode == SONLY) {
      for (int spin = 0; spin < (1 << 22); ++spin) {
        const unsigned f0 = __hip_atomic_load(hfl + 32, __ATOMIC_RELAXED, __HIP_MEMORY_SCOPE_AGENT);
        const unsigned f1 = __hip_atomic_load(hfl + 48, __ATOMIC_RELAXED, __HIP_MEMORY_SCOPE_AGENT);
        if (__builtin_amdgcn_readfirstlane(((f0 - hepoch) ^ 1u) | ((f1 - hepoch) ^ 1u)) == 0u) break;
        __builtin_amdgcn_s_sleep(8);
      }
      __builtin_amdgcn_fence(__ATOMIC_ACQUIRE, "agent");
      asm volatile("" ::: "memory");
      if (lane == 0) __hip_atomic_store(hfl, hepoch + 1u, __ATOMIC_RELAXED, __HIP_MEMORY_SCOPE_AGENT);
      dma_tile_sc1(nxu, dvoff, xr_a);
    }
    {
      u32x4 vp[3][4], idf[2], xin[6], ov[2];
      f32x16 acc[12]; SMState sm;
      const unsigned obase = wid * FRAMEB + (wy * 8) * IMGROWB + (wx * 8) * ROWB;
      sfor<0, 81>(LAM(M) {
        constexpr int m = M.v, ch = m / 6, k = m % 6;
        constexpr int CT[12] = {0, 0, 0, 1, 0, 0, 1, 0, 1, 1, 1, 1};
        constexpr int CA[12] = {0, 1, 2, 0, 3, 4, 1, 5, 3, 2, 4, 5};
        if constexpr (m == 48) {
          if (!SO && more) {
            VMWAIT(2);
#pragma unroll
            for (int s = 0; s < 6; ++s) xin[s] = lds_ld(xl + 32 * s);
            LGWAIT();
            if (nmode == SONLY) dma_tile_sc1(nxu + nstep, dvoff, xr_a); else dma_tile(nxu + nstep, dvoff, xr_a);
          } else {
#pragma unroll
            for (int s = 0; s < 6; ++s) xin[s] = u32x4{};
          }
        }
        if constexpr (ch < 12) {
          if constexpr (CT[ch] == 0) {
            constexpr int vc = CA[ch], n = 108 + 6 * vc + k;
            acc[ch] = MFMA(h2[vc & 1][k], WUSE(n), k == 0 ? zero : acc[ch]); WNEXT(n);
          } else {
            constexpr int q = CA[ch] / 3, T = CA[ch] % 3;
            if constexpr (k < 2) acc[ch] = MFMA(idf[k], x1[q][2 * T + k], k == 0 ? zero : acc[ch]);
            else acc[ch] = MFMA(vp[T][k - 2], pf[q][k - 2], acc[ch]);
          }
          SB();
        }
        if constexpr (m == 12) { idf[0] = lds_ld(c.wa + LDS_ID); idf[1] = lds_ld(c.wa + LDS_ID + 1024); }
        if constexpr (m >= 2 && m < 42) {
          sfor<sched_cnt(m, 2, 42, 30), sched_cnt(m + 1, 2, 42, 30)>(LAM(U) { sm_unit<U.v>(sa[1][0], sa[1][1], sm, pf[1]); });
        }
        if constexpr (ch >= 1 && ch <= 12) {
          constexpr int pc = ch - 1;
          if constexpr (CT[pc] == 0) {
            if constexpr (k == 2 || k == 3) cvt4(vp[CA[pc] >> 1][2 * (CA[pc] & 1) + k - 2], acc[pc], k - 2);
          } else {
            constexpr int ob = (pc & 1) * 2048;
            if constexpr (k == 2) { u32x4 a; cvt4(a, acc[pc], 0); lds_st(ow0 + ob, a); }
            if constexpr (k == 3) { u32x4 a; cvt4(a, acc[pc], 1); lds_st(ow1 + ob, a); asm volatile("" ::: "memory"); }
            if constexpr (k == 4) { ov[0] = lds_ld(ord + ob); ov[1] = lds_ld(ord + ob + 1024); asm volatile("" ::: "memory"); }
          }
        }
        if constexpr (ch >= 2 && k == 1) {
          constexpr int pc = ch - 2;
          if constexpr (CT[pc] == 1) {
            constexpr int q = CA[pc] / 3, T = CA[pc] % 3;
            SB();
            __builtin_amdgcn_raw_buffer_store_b128(ov[0], orsrc, ooff, obase + q * IMGROWB + 64 * T, STORE_AUX);
            __builtin_amdgcn_raw_buffer_store_b128(ov[1], orsrc, ooff, obase + (4 + q) * IMGROWB + 64 * T, STORE_AUX);
            SB(); asm volatile("s_nop 1" ::: "memory"); SB();
          }
        }
        if constexpr (!SO && m >= 49) {
          stat_pkts<sched_cnt(m, 49, 70, 24), sched_cnt(m + 1, 49, 70, 24)>(xin, xsA, stA);
          if constexpr (m == 70) stat_fin(stA);
          if constexpr (m == 72 || m == 73) { nrm_pkt(xsA, hfA, stA, 2 * (m - 72)); nrm_pkt(xsA, hfA, stA, 2 * (m - 72) + 1); }
        }
        SB();
      });
    }
  };

  VMWAIT(0);
  if (shelp && grp == 1) __builtin_amdgcn_s_setprio(1);
  cold_start2();
  if (thelp) {
    TState t; u32x4 xin[6], hfB[6];
    t.hf[0] = hfA[0];
    temporal_slots<0, 0, 11, 10, false, false>(c, ring, t, xsA, stA, xin, xsB, stB, hfB, xsA, xw);
    VMWAIT(0);
#pragma unroll
    for (int s = 0; s < 6; ++s) xin[s] = lds_ld(xl + 32 * s);
    cnt_arrive(l16, dvoff, cR);
    temporal_slots<0, 11, 48, 10, false, false>(c, ring, t, xsA, stA, xin, xsB, stB, hfB, xsA, xw);
    cnt_wait(l16, cR, 4);
#pragma unroll
    for (int s = 0; s < 6; ++s) lds_st(xw + s * 1024, pack8(xsA[s >> 1], s & 1));
    cnt_arrive(l16, dvoff, cW);
#pragma unroll
    for (int n = 0; n < RD; ++n) ring[n] = wload(c, n);
    cnt_wait(l16, cW, 4);
#pragma unroll
    for (int s = 0; s < 6; ++s) xin[s] = lds_ld(xr + s * 1024);
#pragma unroll
    for (int s = 0; s < 6; ++s) lds_st((lds_char*)xl + 32 * s, xin[s]);
    asm volatile("" ::: "memory");
#pragma unroll
    for (int k = 0; k < 6; ++k) {
      const unsigned q = k * 64 + lane, row = (q * 683u) >> 16, n = q - 96u * row;
      xin[k] = lds_ld(smem + LDS_X + wid8 * XREG + row * XFS + 16 * n);
    }
    LGWAIT();
    {
      const int wy = unit / NWIN, wx = unit % NWIN;
      dma_tile(x + (size_t)(wy * 8 + 2 * wid + 1) * IMGROWB + (size_t)(wx * 8) * ROWB, dvoff, xr_a);
    }
    auto srs = __builtin_amdgcn_make_buffer_rsrc((void*)(ws + SCR_OFF), 0, 4 * FRAMEB, 0x00020000);
#pragma unroll
    for (int k = 0; k < 6; ++k) {
      const unsigned q = k * 64 + lane, row = (q * 683u) >> 16, n = q - 96u * row;
      __builtin_amdgcn_raw_buffer_store_b128(xin[k], srs, row * FRAMEB + 16 * n + (hslot * 2 + ti) * 1536, 0, 16);
    }
#pragma unroll
    for (int T = 0; T < 3; ++T) xsA[T] = xsB[T];
    stA = stB; hfA[0] = hfB[0];
    ph = 4;
  }
  for (;;) {
    next_setup();
    phaseT();
    phaseS(IC<0>{});
    if (nmode != FULL) break;
    ph += 8;
    unit = nunit; nmode = NONE;
    first = false; cold = false;
  }
  if (nmode == SONLY) {
    unit = nunit;
    __builtin_amdgcn_s_setprio(0);
#pragma unroll
    for (int T = 0; T < 3; ++T) xsC[T] = xsA[T];
    stC = stA; h2[0][0] = hfA[0];
#pragma unroll
    for (int s = 0; s < 6; ++s) x1[0][s] = pack8(xsA[s >> 1], s & 1);
#pragma unroll
    for (int n = 0; n < RD; ++n) ring[n] = wload(c, 72 + n);
    phaseS(IC<1>{});
  }
}

extern "C" void kernel_launch(void* const* d_in, const int* in_sizes, int n_in, void* d_out, int out_size,
                              void* d_ws, size_t ws_size, hipStream_t stream) {
  (void)hipFuncSetAttribute((const void*)swin_kernel, hipFuncAttributeMaxDynamicSharedMemorySize, LDS_TOTAL);
  swin_kernel<<<NWG, 512, LDS_TOTAL, stream>>>((const char*)d_in[0], (const char*)d_in[1], (const char*)d_in[2],
                                               (const char*)d_in[3], (const char*)d_in[4], (char*)d_out, (char*)d_ws);
}
```
